```python
import torch

def _softmax(x, axis=-1):
    return torch.softmax(x, dim=axis)
def _gelu(x):
    return torch.nn.functional.gelu(x, approximate="tanh")
def _silu(x):
    return torch.nn.functional.silu(x)
def _sigmoid(x):
    return torch.sigmoid(x)
def _layernorm(x, axis=-1, eps=1e-5):
    m = x.mean(dim=axis, keepdim=True)
    v = x.var(dim=axis, keepdim=True, unbiased=False)
    return (x - m) / torch.sqrt(v + eps)
def _scatter_add(tgt, idx, src, axis=0):
    return tgt.clone().index_add_(axis, idx, src)
def _segment_sum(src, seg, n):
    out = torch.zeros((n,) + src.shape[1:], dtype=src.dtype)
    return out.index_add_(0, seg, src)
def _pad(x, pw):
    flat = tuple(v for pair in reversed(pw) for v in pair)
    return torch.nn.functional.pad(x, flat)

kernel_name = 'gated_mix__ssm_conv_h3__dense_activation_gelu__prompt'

def setup_inputs():

    _w = (lambda a: (lambda t: t.to(torch.bfloat16) if t.is_floating_point() else t)(torch.as_tensor(a)))
    def _A_setup():
        B, S, D = 1, 2048, 512
        ks = 4
        return {
            "q": _w((lambda *s: torch.randn(*s, dtype=torch.bfloat16) * 0.05)(B, S, D)),
            "k": _w((lambda *s: torch.randn(*s, dtype=torch.bfloat16) * 0.2)(B, S, D)),
            "v": _w((lambda *s: torch.randn(*s, dtype=torch.bfloat16) * 0.2)(B, S, D)),
            "shift_k": _w((lambda *s: torch.randn(*s, dtype=torch.bfloat16) * 0.2)(D, ks) * 0.5),
            "ssm_k": _w((lambda *s: torch.randn(*s, dtype=torch.bfloat16) * 0.8)(D, S) * (1.0 / S ** 0.5)),
            "ssm_D": _w((lambda *s: torch.randn(*s, dtype=torch.bfloat16) * 0.2)(D)),
        }
    _A_in = _A_setup()

    def _B_setup():
        B, S, D, F = 1, 2048, 512, 1376
        return {
            "x": _w((lambda *s: torch.randn(*s, dtype=torch.bfloat16) * 0.05)(B, S, D)),
            "w1": _w((lambda *s: torch.randn(*s, dtype=torch.bfloat16) * 3.2)(D, F) * (D ** -0.5)),
            "b1": _w((lambda *s: torch.randn(*s, dtype=torch.bfloat16) * 3.2)(F) * 0.01),
            "w2": _w((lambda *s: torch.randn(*s, dtype=torch.bfloat16) * 0.05)(F, D) * (F ** -0.5)),
            "b2": _w((lambda *s: torch.randn(*s, dtype=torch.bfloat16) * 0.1)(D) * 0.01),
        }
    _B_in = _B_setup()

    return {
        "A_q": _A_in["q"],
        "A_k": _A_in["k"],
        "A_v": _A_in["v"],
        "A_shift_k": _A_in["shift_k"],
        "A_ssm_k": _A_in["ssm_k"],
        "A_ssm_D": _A_in["ssm_D"],
        "B_x": _B_in["x"],
        "B_w1": _B_in["w1"],
        "B_b1": _B_in["b1"],
        "B_w2": _B_in["w2"],
        "B_b2": _B_in["b2"],
        "gate_w": _w((lambda *s: torch.randn(*s, dtype=torch.bfloat16) * 0.05)(512) * 16.0),
    }

def reference(A_q, A_k, A_v, A_shift_k, A_ssm_k, A_ssm_D, B_x, B_w1, B_b1, B_w2, B_b2, gate_w):
    def _A_ref(q, k, v, shift_k, ssm_k, ssm_D):
        B, S, D = q.shape
        ks = shift_k.shape[-1]
        k_pad = _pad(k, ((0, 0), (ks - 1, 0), (0, 0)))
        k_shift = torch.zeros_like(k)
        for t in range(ks):
            k_shift = k_shift + k_pad[:, t:t + S, :] * shift_k[:, ks - 1 - t][None, None, :]
        kv = k_shift * v
        L = 2 * S
        kv_pad = _pad(kv, ((0, 0), (0, S), (0, 0)))
        kf_pad = _pad(ssm_k, ((0, 0), (0, S)))
        kv_f = (lambda x, axes=(-2,-1): torch.fft.rfft2(x.float(), dim=axes))(kv_pad[:, None, :, :], axes=(1, 2))
        kf_f = (lambda x, axes=(-2,-1): torch.fft.rfft2(x.float(), dim=axes))(kf_pad[:, None, :], axes=(1, 2))
        kf_f = (lambda x, *d: x.permute(*d))(kf_f, 1, 2, 0)
        y_f = kv_f * kf_f[None, :, :, :]
        y = (lambda x, s, axes=(-2,-1): torch.fft.irfft2(x, s=s, dim=axes).to(torch.bfloat16))(y_f, (1, L), axes=(1, 2))[:, 0, :S, :]
        y = y + kv * ssm_D[None, None, :]
        return q * y

    def _B_ref(x, w1, b1, w2, b2):
        h = torch.einsum("bsd,df->bsf", x, w1) + b1
        h = _gelu(h)
        return torch.einsum("bsf,fd->bsd", h, w2) + b2

    x = A_q
    g = _sigmoid((x * gate_w).sum(-1, keepdims=True))
    a = _A_ref(x, A_k, A_v, A_shift_k, A_ssm_k, A_ssm_D)
    b = _B_ref(x, B_w1, B_b1, B_w2, B_b2)
    return g * a + (1 - g) * b
```

```cpp
#include <hip/hip_runtime.h>
#include <hip/hip_bf16.h>

typedef unsigned u32;
typedef unsigned long long u64;
typedef __attribute__((ext_vector_type(2))) u32 u32x2;
typedef __attribute__((ext_vector_type(4))) u32 u32x4;
typedef __attribute__((ext_vector_type(4))) float f32x4;
typedef __attribute__((ext_vector_type(2))) float f32x2;
typedef __attribute__((ext_vector_type(8))) __bf16 bf16x8;
typedef __attribute__((ext_vector_type(2))) __bf16 bf16x2;
typedef __attribute__((ext_vector_type(4))) short s16x4;
#define LAS __attribute__((address_space(3)))
#define GAS __attribute__((address_space(1)))
#define DEV __device__ __forceinline__

constexpr int S_ = 2048, D_ = 512, F_ = 1376, NSTEP = 43;
constexpr size_t WS_EPOCH = 0, WS_FA = 4096, WS_FH = 8192, WS_FZ = 12288;
constexpr size_t WS_KVT = 65536, WS_ZT = WS_KVT + 2097152, WS_H = WS_ZT + 2097152;
constexpr u32 H_BYTES = 128u * NSTEP * 1024u;
constexpr size_t WS_DBG = WS_H + H_BYTES;
constexpr int SLOT = 28672, L_W1 = 16384;
constexpr int L_R = 4 * SLOT, RP = 4160, RCH = 4 * RP;
constexpr int L_U = L_R + 2 * RCH, UCH = 4608;
constexpr int L_GW = L_U + 2 * UCH;
constexpr int L_MISC = L_GW + 1024;
constexpr int L_TOTAL = L_MISC + 64;
constexpr int SPIN_MAX = 1 << 18;

#define TS(i)

struct Params {
  const char *q, *k, *v, *shift_k, *ssm_k, *ssm_D, *w1, *b1, *w2, *b2, *gate_w;
  char *out, *ws;
};

DEV void dma16(const char* sbase, u32 voff, u32 lds) {
  asm volatile("s_mov_b32 m0, %2\n\ts_nop 0\n\tglobal_load_lds_dwordx4 %0, %1" :: "v"(voff), "s"(sbase), "s"(lds) : "memory");
}
template <int N> DEV void wait_vm() { asm volatile("s_waitcnt vmcnt(%0)" :: "n"(N) : "memory"); }
DEV void wait_lgkm0() { asm volatile("s_waitcnt lgkmcnt(0)" ::: "memory"); }
DEV void bar() { asm volatile("s_waitcnt lgkmcnt(0)\n\ts_barrier" ::: "memory"); }
DEV u32x4 lds128(u32 a) { return *(LAS u32x4*)a; }
DEV u32x2 lds64(u32 a) { return *(LAS u32x2*)a; }
DEV u32 lds32(u32 a) { return *(LAS u32*)a; }
DEV u32x2 ldstr(u32 a) { return __builtin_bit_cast(u32x2, __builtin_amdgcn_ds_read_tr16_b64_v4i16((LAS s16x4*)a)); }
DEV u32x4 cat(u32x2 a, u32x2 b) { u32x4 r = {a.x, a.y, b.x, b.y}; return r; }
DEV f32x4 mfma(u32x4 a, u32x4 b, f32x4 c) {
  return __builtin_amdgcn_mfma_f32_16x16x32_bf16(__builtin_bit_cast(bf16x8, a), __builtin_bit_cast(bf16x8, b), c, 0, 0, 0);
}
DEV f32x4 sel4(int c, f32x4 a, f32x4 b) { f32x4 r = {c ? a.x : b.x, c ? a.y : b.y, c ? a.z : b.z, c ? a.w : b.w}; return r; }
DEV float blo(u32 x) { return __builtin_bit_cast(float, x << 16); }
DEV float bhi(u32 x) { return __builtin_bit_cast(float, x & 0xffff0000u); }
DEV u32 pk(float a, float b) { bf16x2 r = {(__bf16)a, (__bf16)b}; return __builtin_bit_cast(u32, r); }
DEV float dot2(u32 a, u32 b, float c) {
  return __builtin_amdgcn_fdot2_f32_bf16(__builtin_bit_cast(bf16x2, a), __builtin_bit_cast(bf16x2, b), c, false);
}
DEV float gelu(float x) {
  const float c1 = -2.302208198f, c2 = -0.1029432397f;
  float e = __builtin_amdgcn_exp2f(x * (c1 + c2 * x * x));
  return x * __builtin_amdgcn_rcpf(1.0f + e);
}
DEV void publish(char* flag, u64 tag) { __hip_atomic_store((GAS u64*)flag, tag, __ATOMIC_RELAXED, __HIP_MEMORY_SCOPE_AGENT); }
DEV void poll(const char* flag, u64 tag) {
#pragma unroll 1
  for (int i = 0; i < SPIN_MAX; ++i) {
    u64 x = __hip_atomic_load((GAS u64*)flag, __ATOMIC_RELAXED, __HIP_MEMORY_SCOPE_AGENT);
    if (__all(x == tag)) break;
    __builtin_amdgcn_s_sleep(2);
  }
  asm volatile("" ::: "memory");
}
DEV u32 lds_arrive(u32 a) {
  u32 old = 0;
  wait_lgkm0();
  if ((threadIdx.x & 63) == 0) old = __hip_atomic_fetch_add((LAS u32*)a, 1u, __ATOMIC_RELAXED, __HIP_MEMORY_SCOPE_WORKGROUP);
  return __builtin_amdgcn_readfirstlane(old);
}
DEV void lds_wait(u32 a, u32 target) {
#pragma unroll 1
  for (int i = 0; i < SPIN_MAX; ++i) {
    u32 x = __hip_atomic_load((LAS u32*)a, __ATOMIC_RELAXED, __HIP_MEMORY_SCOPE_WORKGROUP);
    if (__builtin_amdgcn_readfirstlane(x) >= target) break;
    __builtin_amdgcn_s_sleep(1);
  }
  asm volatile("" ::: "memory");
}

__global__ void __launch_bounds__(512, 2) fused_kernel(Params p) {
  extern __shared__ __attribute__((aligned(16))) char smem[];
  const u32 sm = (u32)(size_t)(LAS char*)smem;
  const int b = blockIdx.x, l = threadIdx.x & 63;
  const int wid = __builtin_amdgcn_readfirstlane(threadIdx.x >> 6), w = wid & 3;
  const int xcd = b & 7, jj = b >> 3, cl = xcd * 2 + (jj >> 4), mem = jj & 15;
  const int rh = mem & 1, ct = mem >> 1;
  const int r1 = cl * 128, r2 = r1 + rh * 64;
  const int st = (mem == 15) ? 41 : (mem >> 2) * 11 + (mem & 3) * 3;
  const int nst = ((mem & 3) == 3 || mem == 14) ? 2 : 3;
  const int i16 = l & 15, g = l >> 4;
  const u32 cntC = sm + L_MISC, cntW = cntC + 4, cntH = cntC + 8, cntZ = cntC + 12, ltag = cntC + 16;
  const __amdgpu_buffer_rsrc_t rws = __builtin_amdgcn_make_buffer_rsrc(p.ws, 0, (u32)WS_DBG, 0x00020000);
  TS(0);

  if (wid < 4) {
    for (int pc = w; pc < 17; pc += 4) {
      int row = r2 + (pc < 9 ? pc - 1 : pc - 9) * 8 + (l >> 3);
      row = row < 0 ? 0 : row;
      int sw = (pc < 9 ? pc : pc - 9) & 3;
      dma16(pc < 9 ? p.k : p.v, row * 1024 + ct * 128 + (((l & 7) ^ (2 * sw)) * 16), sm + L_R + pc * 1024);
    }
    const int cp = l & 7, rg = l >> 3, c0 = ct * 64 + w * 16 + cp * 2;
    u32x4 sk = *(const u32x4*)(p.shift_k + c0 * 8);
    f32x4 acc[6][2];
#pragma unroll
    for (int fb = 0; fb < 6; ++fb) {
      int f = st * 32 + fb * 16 + 4 * g;
      f = f < F_ ? f : 0;
      u32x2 bb = *(const u32x2*)(p.b1 + f * 2);
      f32x4 t = {blo(bb.x), bhi(bb.x), blo(bb.y), bhi(bb.y)};
      acc[fb][0] = t; acc[fb][1] = t;
    }
    if (wid == 0 && l == 0) {
      u64 e = __hip_atomic_load((GAS u64*)(p.ws + WS_EPOCH), __ATOMIC_RELAXED, __HIP_MEMORY_SCOPE_AGENT);
      *(LAS u64*)ltag = e + 1;
      *(LAS u32x4*)cntC = u32x4{0, 0, 0, 0};
    }
    wait_vm<0>();
    bar();
    TS(1);
    {
      const u32 col = (w * 2 + (cp >> 2)) * 16, in = (cp & 3) * 4;
      const u32 a0 = sm + L_R + rg * 1024 + ((col ^ ((rg & 3) * 32)) + in);
      const u32 a1 = sm + L_R + (rg + 1) * 1024 + ((col ^ (((rg + 1) & 3) * 32)) + in);
      u32 kk[11], vv[8];
#pragma unroll
      for (int i = 0; i < 3; ++i) { kk[i] = lds32(a0 + (5 + i) * 128); if (r2 == 0 && rg == 0) kk[i] = 0; }
#pragma unroll
      for (int i = 0; i < 8; ++i) { kk[3 + i] = lds32(a1 + i * 128); vv[i] = lds32(a0 + 9 * 1024 + i * 128); }
      float x0[8], x1[8];
#pragma unroll
      for (int i = 0; i < 8; ++i) {
        x0[i] = (blo(kk[i]) * bhi(sk.y) + blo(kk[i + 1]) * blo(sk.y) + blo(kk[i + 2]) * bhi(sk.x) + blo(kk[i + 3]) * blo(sk.x)) * blo(vv[i]);
        x1[i] = (bhi(kk[i]) * bhi(sk.w) + bhi(kk[i + 1]) * blo(sk.w) + bhi(kk[i + 2]) * bhi(sk.z) + bhi(kk[i + 3]) * blo(sk.z)) * bhi(vv[i]);
      }
      u32x4 o0 = {pk(x0[0], x0[1]), pk(x0[2], x0[3]), pk(x0[4], x0[5]), pk(x0[6], x0[7])};
      u32x4 o1 = {pk(x1[0], x1[1]), pk(x1[2], x1[3]), pk(x1[4], x1[5]), pk(x1[6], x1[7])};
      const u32 off = (u32)WS_KVT + c0 * 4096 + (r2 + rg * 8) * 2;
      __builtin_amdgcn_raw_buffer_store_b128(o0, rws, off, 0, 16);
      __builtin_amdgcn_raw_buffer_store_b128(o1, rws, off + 4096, 0, 16);
    }
    const u64 tag = *(LAS u64*)ltag;
    TS(2);
    const int xr = w * 32 + i16;
    const u32 xa0 = sm + xr * 128 + ((g ^ ((xr >> 1) & 7)) * 16);
    const u32 wa0 = sm + L_W1 + (8 * g + (i16 >> 2)) * 192 + (l & 3) * 8 + (g & 1) * 32;
    const u32 wa1 = wa0 + 32 - (g & 1) * 64;
#pragma unroll 1
    for (int s = 0; s < 8; ++s) {
      if (s == 2) wait_vm<0>();
      bar();
      if (s == 2 && wid == 0 && l == 0) publish(p.ws + WS_FA + (ct * 32 + (r2 >> 6)) * 8, tag);
      const u32 so = (s & 3) * SLOT;
#pragma unroll
      for (int ks = 0; ks < 2; ++ks) {
        u32x4 xb0 = lds128(((xa0 ^ (ks * 64)) + so)), xb1 = lds128((xa0 ^ (ks * 64)) + so + 2048);
#pragma unroll
        for (int fb = 0; fb < 6; ++fb) {
          const u32 a = ((fb & 1) ? wa1 : wa0) + so + ks * 6144 + (fb >> 1) * 64;
          u32x4 wf = cat(ldstr(a), ldstr(a + 768));
          acc[fb][0] = mfma(wf, xb0, acc[fb][0]);
          acc[fb][1] = mfma(wf, xb1, acc[fb][1]);
        }
      }
    }
    bar();
    TS(3);
#pragma unroll
    for (int u = 0; u < 3; ++u) {
      if (u < nst) {
#pragma unroll
        for (int sb = 0; sb < 2; ++sb) {
          f32x4 a = acc[2 * u][sb], c = acc[2 * u + 1][sb];
          u32x4 o = {pk(gelu(a.x), gelu(a.y)), pk(gelu(a.z), gelu(a.w)), pk(gelu(c.x), gelu(c.y)), pk(gelu(c.z), gelu(c.w))};
          __builtin_amdgcn_raw_buffer_store_b128(o, rws, (u32)WS_H + ((cl * 8 + w * 2 + sb) * NSTEP + st + u) * 1024 + l * 16, 0, 16);
        }
      }
    }
    TS(4);
    wait_vm<0>();
    if (lds_arrive(cntH) == 3 && l == 0) publish(p.ws + WS_FH + (cl * 16 + mem) * 8, tag);
    TS(5);
    lds_wait(cntW, 4);
    const u32 row2 = 4 * g + (i16 >> 2);
    const u32 va = sm + w * SLOT + row2 * 128 + (l & 3) * 8, vsw = ((row2 >> 1) & 3) * 32;
    u32x4 wr[4][4];
#pragma unroll
    for (int i = 0; i < 4; ++i)
#pragma unroll
      for (int nb = 0; nb < 4; ++nb) { const u32 a = va + ((nb * 32) ^ vsw) + i * 4096; wr[i][nb] = cat(ldstr(a), ldstr(a + 2048)); }
#pragma unroll
    for (int i = 0; i < 4; ++i)
#pragma unroll
      for (int nb = 0; nb < 4; ++nb) asm volatile("" : "+v"(wr[i][nb]));
    {
      const u32 vo = (l >> 3) * 1024 + ct * 128 + (((l & 7) ^ (2 * ((l >> 4) & 3))) * 16);
#pragma unroll 1
      for (int i = 0; i < 16; ++i) {
        int row = (w * 11 + 7) * 32 + i * 8;
        row = row < F_ ? row : 0;
        dma16(p.w2 + row * 1024, vo, sm + w * SLOT + i * 1024);
      }
    }
    TS(6);
    poll(p.ws + WS_FH + (cl * 16 + w * 4 + (l & 3)) * 8, tag);
    TS(7);
    f32x4 ac2[4][4];
#pragma unroll
    for (int nb = 0; nb < 4; ++nb)
#pragma unroll
      for (int sb = 0; sb < 4; ++sb) ac2[nb][sb] = f32x4{0, 0, 0, 0};
    const u32 ho = (u32)WS_H + ((cl * 8 + rh * 4) * NSTEP + w * 11) * 1024 + l * 16;
    u32x4 hf[11][4];
    constexpr int WIN = 5;
#pragma unroll
    for (int i = 0; i < WIN; ++i)
#pragma unroll
      for (int sb = 0; sb < 4; ++sb) hf[i][sb] = __builtin_amdgcn_raw_buffer_load_b128(rws, ho + i * 1024, sb * NSTEP * 1024, 16);
#pragma unroll
    for (int i = 0; i < 11; ++i) {
      if (i + WIN < 11) {
        const u32 o = (i + WIN == 10 && w == 3) ? 0x80000000u : ho + (i + WIN) * 1024;
#pragma unroll
        for (int sb = 0; sb < 4; ++sb) hf[i + WIN][sb] = __builtin_amdgcn_raw_buffer_load_b128(rws, o, sb * NSTEP * 1024, 16);
      }
      if (i == 4) wait_vm<24>();
      __builtin_amdgcn_sched_barrier(0);
#pragma unroll
      for (int nb = 0; nb < 4; ++nb) {
        u32x4 wf;
        if (i < 4) wf = wr[i][nb];
        else { const u32 a = va + ((nb * 32) ^ vsw) + (i < 7 ? i : i - 7) * 4096; wf = cat(ldstr(a), ldstr(a + 2048)); }
#pragma unroll
        for (int sb = 0; sb < 4; ++sb) ac2[nb][sb] = mfma(wf, hf[i][sb], ac2[nb][sb]);
      }
    }
    TS(8);
    wait_lgkm0();
#pragma unroll
    for (int nb = 0; nb < 4; ++nb)
#pragma unroll
      for (int sb = 0; sb < 4; ++sb)
        *(LAS f32x4*)(sm + w * SLOT + (sb * 16 + i16) * 256 + (((4 * nb + g) ^ (l & 7)) * 16)) = ac2[nb][sb];
    bar();
    TS(9);
    if (b == 0 && wid == 0) {
#pragma unroll 1
      for (int i = 0; i < 4; ++i) poll(p.ws + WS_FA + (i * 64 + l) * 8, tag);
      if (l == 0) publish(p.ws + WS_EPOCH, tag);
    }
  } else {
    const int ch = w >> 1, hf = w & 1, chan = (xcd * 32 + jj) * 2 + ch, tc = w * 64 + l;
    u32 vx[4], vw[3];
#pragma unroll
    for (int i = 0; i < 4; ++i) { int r = (w * 4 + i) * 8 + (l >> 3); vx[i] = (r1 + r) * 1024 + (((l & 7) ^ ((r >> 1) & 7)) * 16); }
#pragma unroll
    for (int i = 0; i < 3; ++i) {
      int idx = (w * 3 + i) * 64 + l, r = idx / 12, c = (idx - r * 12) ^ (2 * ((r >> 3) & 1));
      if (c >= nst * 4) c -= 4;
      vw[i] = r * (F_ * 2) + st * 64 + c * 16;
    }
    auto stage = [&](int s) {
      const u32 so = sm + (s & 3) * SLOT;
#pragma unroll
      for (int i = 0; i < 4; ++i) dma16(p.q + s * 128, vx[i], so + (w * 4 + i) * 1024);
#pragma unroll
      for (int i = 0; i < 3; ++i) dma16(p.w1 + s * (64 * F_ * 2), vw[i], so + L_W1 + (w * 3 + i) * 1024);
    };
    dma16(p.gate_w, l * 16, sm + L_GW);
    stage(0);
    u32x4 o1[2]; u32x2 o2[2];
#pragma unroll
    for (int i = 0; i < 2; ++i) {
      const int a = hf * 64 + l + 128 * i;
      asm volatile("global_load_dwordx4 %0, %1, %2" : "=v"(o1[i]) : "v"(chan * 4096 + (2040 - 8 * a) * 2), "s"(p.ssm_k) : "memory");
      asm volatile("global_load_dwordx2 %0, %1, %2" : "=v"(o2[i]) : "v"(chan * 4096 + (a == 0 ? 0 : (2048 - 8 * a) * 2)), "s"(p.ssm_k) : "memory");
    }
    const float dsk = blo(*(const unsigned short*)(p.ssm_D + chan * 2));
    const u32x4 b2v = *(const u32x4*)(p.b2 + ct * 128 + (l & 7) * 16);
    asm volatile("" ::: "memory");
    stage(1); stage(2); stage(3);
    if (l < 32 && hf == 0) *(LAS u32x4*)(sm + L_U + ch * UCH + l * 16) = u32x4{0, 0, 0, 0};
    bar();
    const u64 tag = *(LAS u64*)ltag;
    const int er = tc >> 3, ec = tc & 7;
    const u32 ga = sm + (rh * 64 + er) * 128 + ((ec ^ ((er >> 1) & 7)) * 16);
    float gs0 = 0.f, gs1 = 0.f;
    u32x4 q0 = {0, 0, 0, 0}, q1 = {0, 0, 0, 0};
    const u32 vo2 = (l >> 3) * 1024 + ct * 128 + (((l & 7) ^ (2 * ((l >> 4) & 3))) * 16);
#pragma unroll 1
    for (int s = 0; s < 9; ++s) {
      if (s == 0) wait_vm<21>(); else wait_vm<14>();
      bar();
      if (s >= 1 && s <= 4) stage(s + 3);
      if (s >= 5) {
        const int fw = (s + 3) & 3;
#pragma unroll
        for (int i = 0; i < 7; ++i) dma16(p.w2 + (fw * 11 * 32 + (w * 7 + i) * 8) * 1024, vo2, sm + fw * SLOT + (w * 7 + i) * 1024);
      }
      if (s < 8) {
        const u32 so = (s & 3) * SLOT;
        u32x4 a0 = lds128(ga + so), a1 = lds128(ga + so + 4096), gw = lds128(sm + L_GW + s * 128 + ec * 16);
#pragma unroll
        for (int i = 0; i < 4; ++i) { gs0 = dot2(a0[i], gw[i], gs0); gs1 = dot2(a1[i], gw[i], gs1); }
        if (s == ct) { q0 = a0; q1 = a1; }
      }
      if (s == 0) {
        asm volatile("" : "+v"(o1[0]), "+v"(o1[1]), "+v"(o2[0]), "+v"(o2[1]));
        const u32 rb = sm + L_R + ch * RCH;
#pragma unroll
        for (int i = 0; i < 3; ++i) {
          u32 d[6];
          if (i < 2) { d[0] = o1[i].x; d[1] = o1[i].y; d[2] = o1[i].z; d[3] = o1[i].w; d[4] = o2[i].x; d[5] = o2[i].y; }
          else { d[0] = d[1] = d[2] = d[3] = 0; d[4] = l == 0 ? o2[0].x : 0; d[5] = l == 0 ? o2[0].y : 0; }
          u32 ro[6], mx[5];
#pragma unroll
          for (int t = 1; t < 6; ++t) { ro[t] = __builtin_amdgcn_alignbit(d[t], d[t], 16); mx[t - 1] = (d[t] & 0xffffu) | (d[t - 1] & 0xffff0000u); }
          const u32 a = rb + (i < 2 ? hf * 64 + l + 128 * i : 256 + l) * 16;
          if (i < 2 || (hf == 0 && l < 2)) {
            *(LAS u32x4*)(a) = u32x4{mx[3], mx[2], mx[1], mx[0]};
            *(LAS u32x4*)(a + RP) = u32x4{ro[4], ro[3], ro[2], ro[1]};
            *(LAS u32x4*)(a + 2 * RP) = u32x4{mx[4], mx[3], mx[2], mx[1]};
            *(LAS u32x4*)(a + 3 * RP) = u32x4{ro[5], ro[4], ro[3], ro[2]};
          }
        }
      }
    }
    TS(1);
    poll(p.ws + WS_FA + (xcd * 32 + (l & 31)) * 8, tag);
    {
      const u32 off = (u32)WS_KVT + chan * 4096 + hf * 2048 + l * 16;
      u32x4 u0 = __builtin_amdgcn_raw_buffer_load_b128(rws, off, 0, 16), u1 = __builtin_amdgcn_raw_buffer_load_b128(rws, off + 1024, 0, 16);
      const u32 a = sm + L_U + ch * UCH + 512 + hf * 2048 + l * 16;
      *(LAS u32x4*)a = u0; *(LAS u32x4*)(a + 1024) = u1;
    }
    wait_vm<0>();
    lds_arrive(cntW);
    lds_wait(cntW, 4);
    TS(2);
    f32x4 y[8];
#pragma unroll
    for (int n = 0; n < 8; ++n) y[n] = f32x4{0, 0, 0, 0};
    {
      u32 ra = sm + L_R + ch * RCH + (i16 & 3) * RP + (2032 - 1792 - 32 * (4 * hf + 3) + 8 * g - (i16 & 12)) * 2;
      u32 rb2 = ra + 8;
      asm volatile("" : "+v"(rb2));
      const u32 ua = sm + L_U + ch * UCH + (256 + 16 * i16 + 8 * g - 16 - 32 * (4 * hf + 3)) * 2;
#pragma unroll
      for (int cc = 0; cc < 4; ++cc) {
        u32x4 A[8], B[8];
#pragma unroll
        for (int n = 0; n < 8; ++n) {
          A[n] = cat(lds64(ra + (7 - n) * 512 + cc * 64), lds64(rb2 + (7 - n) * 512 + cc * 64));
          B[n] = lds128(ua + n * 512 + cc * 64);
        }
#pragma unroll
        for (int n = 0; n < 8; ++n)
#pragma unroll
          for (int pp = 0; pp <= n; ++pp) y[n] = mfma(A[pp], B[n - pp], y[n]);
      }
    }
    TS(3);
    lds_arrive(cntC); lds_wait(cntC, 4);
#pragma unroll
    for (int n = 0; n < 4; ++n) {
      const f32x4 lo = y[n], hi = y[n + 4];
      *(LAS f32x4*)(sm + L_R + (w * 4 + n) * 1024 + l * 16) = sel4(hf, lo, hi);
      y[n] = sel4(hf, hi, lo);
    }
    lds_arrive(cntC); lds_wait(cntC, 8);
#pragma unroll
    for (int n = 0; n < 4; ++n) {
      f32x4 t = *(LAS f32x4*)(sm + L_R + ((w ^ 1) * 4 + n) * 1024 + l * 16) + y[n];
      const int t0 = (hf * 4 + n) * 256 + 16 * i16 + 4 * g;
      u32x2 uu = lds64(sm + L_U + ch * UCH + 512 + t0 * 2);
      u32x2 o = {pk(t.x + blo(uu.x) * dsk, t.y + bhi(uu.x) * dsk), pk(t.z + blo(uu.y) * dsk, t.w + bhi(uu.y) * dsk)};
      __builtin_amdgcn_raw_buffer_store_b64(o, rws, (u32)WS_ZT + chan * 4096 + t0 * 2, 0, 16);
    }
    wait_vm<0>();
    if (lds_arrive(cntZ) == 3 && l == 0) publish(p.ws + WS_FZ + (xcd * 32 + jj) * 8, tag);
    TS(4);
#pragma unroll
    for (int o = 1; o < 8; o <<= 1) { gs0 += __shfl_xor(gs0, o); gs1 += __shfl_xor(gs1, o); }
    const float g0 = __builtin_amdgcn_rcpf(1.0f + __builtin_amdgcn_exp2f(-1.442695041f * gs0));
    const float g1 = __builtin_amdgcn_rcpf(1.0f + __builtin_amdgcn_exp2f(-1.442695041f * gs1));
    poll(p.ws + WS_FZ + (ct * 32 + (l & 31)) * 8, tag);
    TS(5);
    const u32 zb = sm + L_R + 16384;
#pragma unroll
    for (int i = 0; i < 2; ++i) {
      u32x4 z = __builtin_amdgcn_raw_buffer_load_b128(rws, (u32)WS_ZT + (ct * 64 + er + 32 * i) * 4096 + r2 * 2 + ec * 16, 0, 16);
#pragma unroll
      for (int t = 0; t < 4; ++t) *(LAS u32*)(zb + (er + 32 * i) * 132 + ec * 16 + t * 4) = z[t];
    }
    lds_arrive(cntC); lds_wait(cntC, 12);
    float tt[2][8];
#pragma unroll
    for (int i = 0; i < 8; ++i) {
      const u32 a = zb + (ec * 8 + i) * 132 + er * 2;
      const float z0 = blo(*(LAS unsigned short*)a), z1 = blo(*(LAS unsigned short*)(a + 64));
      const float bb = (i & 1) ? bhi(b2v[i >> 1]) : blo(b2v[i >> 1]);
      tt[0][i] = g0 * ((i & 1) ? bhi(q0[i >> 1]) : blo(q0[i >> 1])) * z0 + (1.0f - g0) * bb;
      tt[1][i] = g1 * ((i & 1) ? bhi(q1[i >> 1]) : blo(q1[i >> 1])) * z1 + (1.0f - g1) * bb;
    }
#pragma unroll
    for (int i = 0; i < 8; ++i) asm volatile("" : "+v"(tt[0][i]), "+v"(tt[1][i]));
    TS(6);
    bar();
#pragma unroll
    for (int i = 0; i < 2; ++i) {
      const u32 a = sm + (er + 32 * i) * 256;
      f32x4 s0 = {0, 0, 0, 0}, s1 = {0, 0, 0, 0};
#pragma unroll
      for (int k = 0; k < 4; ++k) {
        s0 += *(LAS f32x4*)(a + k * SLOT + (((2 * ec) ^ (er & 7)) * 16));
        s1 += *(LAS f32x4*)(a + k * SLOT + (((2 * ec + 1) ^ (er & 7)) * 16));
      }
      const float m = 1.0f - (i ? g1 : g0);
      u32x4 o = {pk(tt[i][0] + m * s0.x, tt[i][1] + m * s0.y), pk(tt[i][2] + m * s0.z, tt[i][3] + m * s0.w),
                 pk(tt[i][4] + m * s1.x, tt[i][5] + m * s1.y), pk(tt[i][6] + m * s1.z, tt[i][7] + m * s1.w)};
      *(u32x4*)(p.out + (r2 + er + 32 * i) * 1024 + ct * 128 + ec * 16) = o;
    }
    TS(7);
  }
}

extern "C" void kernel_launch(void* const* d_in, const int* in_sizes, int n_in, void* d_out, int out_size,
                              void* d_ws, size_t ws_size, hipStream_t stream) {
  Params p;
  p.q = (const char*)d_in[0]; p.k = (const char*)d_in[1]; p.v = (const char*)d_in[2];
  p.shift_k = (const char*)d_in[3]; p.ssm_k = (const char*)d_in[4]; p.ssm_D = (const char*)d_in[5];
  p.w1 = (const char*)d_in[7]; p.b1 = (const char*)d_in[8]; p.w2 = (const char*)d_in[9];
  p.b2 = (const char*)d_in[10]; p.gate_w = (const char*)d_in[11];
  p.out = (char*)d_out; p.ws = (char*)d_ws;
  (void)hipFuncSetAttribute((const void*)fused_kernel, hipFuncAttributeMaxDynamicSharedMemorySize, L_TOTAL);
  fused_kernel<<<256, 512, L_TOTAL, stream>>>(p);
}
```

```cpp
#include <hip/hip_runtime.h>
#include <hip/hip_bf16.h>

typedef unsigned u32;
typedef unsigned long long u64;
typedef __attribute__((ext_vector_type(2))) u32 u32x2;
typedef __attribute__((ext_vector_type(4))) u32 u32x4;
typedef __attribute__((ext_vector_type(4))) float f32x4;
typedef __attribute__((ext_vector_type(2))) float f32x2;
typedef __attribute__((ext_vector_type(8))) __bf16 bf16x8;
typedef __attribute__((ext_vector_type(2))) __bf16 bf16x2;
typedef __attribute__((ext_vector_type(4))) short s16x4;
#define LAS __attribute__((address_space(3)))
#define GAS __attribute__((address_space(1)))
#define DEV __device__ __forceinline__

constexpr int S_ = 2048, D_ = 512, F_ = 1376, NSTEP = 43;
constexpr size_t WS_EPOCH = 0, WS_FA = 4096, WS_FH = 8192, WS_FZ = 12288;
constexpr size_t WS_KVT = 65536, WS_ZT = WS_KVT + 2097152, WS_H = WS_ZT + 2097152;
constexpr u32 H_BYTES = 128u * NSTEP * 1024u;
constexpr size_t WS_DBG = WS_H + H_BYTES;
constexpr int SLOT = 28672, L_W1 = 16384;
constexpr int L_R = 4 * SLOT, RP = 4160, RCH = 4 * RP;
constexpr int L_U = L_R + 2 * RCH, UCH = 4608;
constexpr int L_GW = L_U + 2 * UCH;
constexpr int L_MISC = L_GW + 1024;
constexpr int L_TOTAL = L_MISC + 64;
constexpr int SPIN_MAX = 1 << 18;

#define TS(i)
#define TC(i)

struct Params {
  const char *q, *k, *v, *shift_k, *ssm_k, *ssm_D, *w1, *b1, *w2, *b2, *gate_w;
  char *out, *ws;
};

DEV void dma16(const char* sbase, u32 voff, u32 lds) {
  asm volatile("s_mov_b32 m0, %2\n\ts_nop 0\n\tglobal_load_lds_dwordx4 %0, %1" :: "v"(voff), "s"(sbase), "s"(lds) : "memory");
}
template <int N> DEV void wait_vm() { asm volatile("s_waitcnt vmcnt(%0)" :: "n"(N) : "memory"); }
DEV void wait_lgkm0() { asm volatile("s_waitcnt lgkmcnt(0)" ::: "memory"); }
DEV void bar() { asm volatile("s_waitcnt lgkmcnt(0)\n\ts_barrier" ::: "memory"); }
DEV u32x4 lds128(u32 a) { return *(LAS u32x4*)a; }
DEV u32x2 lds64(u32 a) { return *(LAS u32x2*)a; }
DEV u32 lds32(u32 a) { return *(LAS u32*)a; }
DEV u32x2 ldstr(u32 a) { return __builtin_bit_cast(u32x2, __builtin_amdgcn_ds_read_tr16_b64_v4i16((LAS s16x4*)a)); }
DEV u32x4 cat(u32x2 a, u32x2 b) { u32x4 r = {a.x, a.y, b.x, b.y}; return r; }
DEV f32x4 mfma(u32x4 a, u32x4 b, f32x4 c) {
  return __builtin_amdgcn_mfma_f32_16x16x32_bf16(__builtin_bit_cast(bf16x8, a), __builtin_bit_cast(bf16x8, b), c, 0, 0, 0);
}
DEV f32x4 sel4(int c, f32x4 a, f32x4 b) { f32x4 r = {c ? a.x : b.x, c ? a.y : b.y, c ? a.z : b.z, c ? a.w : b.w}; return r; }
DEV float blo(u32 x) { return __builtin_bit_cast(float, x << 16); }
DEV float bhi(u32 x) { return __builtin_bit_cast(float, x & 0xffff0000u); }
DEV u32 pk(float a, float b) { bf16x2 r = {(__bf16)a, (__bf16)b}; return __builtin_bit_cast(u32, r); }
DEV float dot2(u32 a, u32 b, float c) {
  return __builtin_amdgcn_fdot2_f32_bf16(__builtin_bit_cast(bf16x2, a), __builtin_bit_cast(bf16x2, b), c, false);
}
DEV float gelu(float x) {
  const float c1 = -2.302208198f, c2 = -0.1029432397f;
  float e = __builtin_amdgcn_exp2f(x * (c1 + c2 * x * x));
  return x * __builtin_amdgcn_rcpf(1.0f + e);
}
DEV void publish(char* flag, u64 tag) { __hip_atomic_store((GAS u64*)flag, tag, __ATOMIC_RELAXED, __HIP_MEMORY_SCOPE_AGENT); }
DEV void poll(const char* flag, u64 tag) {
#pragma unroll 1
  for (int i = 0; i < SPIN_MAX; ++i) {
    u64 x = __hip_atomic_load((GAS u64*)flag, __ATOMIC_RELAXED, __HIP_MEMORY_SCOPE_AGENT);
    if (__all(x == tag)) break;
    __builtin_amdgcn_s_sleep(2);
  }
  asm volatile("" ::: "memory");
}
DEV u32 lds_arrive(u32 a) {
  u32 old = 0;
  wait_lgkm0();
  if ((threadIdx.x & 63) == 0) old = __hip_atomic_fetch_add((LAS u32*)a, 1u, __ATOMIC_RELAXED, __HIP_MEMORY_SCOPE_WORKGROUP);
  return __builtin_amdgcn_readfirstlane(old);
}
DEV void lds_wait(u32 a, u32 target) {
#pragma unroll 1
  for (int i = 0; i < SPIN_MAX; ++i) {
    u32 x = __hip_atomic_load((LAS u32*)a, __ATOMIC_RELAXED, __HIP_MEMORY_SCOPE_WORKGROUP);
    if (__builtin_amdgcn_readfirstlane(x) >= target) break;
    __builtin_amdgcn_s_sleep(1);
  }
  asm volatile("" ::: "memory");
}

__global__ void __launch_bounds__(512, 2) fused_kernel(Params p) {
  extern __shared__ __attribute__((aligned(16))) char smem[];
  const u32 sm = (u32)(size_t)(LAS char*)smem;
  const int b = blockIdx.x, l = threadIdx.x & 63;
  const int wid = __builtin_amdgcn_readfirstlane(threadIdx.x >> 6), w = wid & 3;
  const int xcd = b & 7, jj = b >> 3, cl = xcd * 2 + (jj >> 4), mem = jj & 15;
  const int rh = mem & 1, ct = mem >> 1;
  const int r1 = cl * 128, r2 = r1 + rh * 64;
  const int st = (mem == 15) ? 41 : (mem >> 2) * 11 + (mem & 3) * 3;
  const int nst = ((mem & 3) == 3 || mem == 14) ? 2 : 3;
  const int i16 = l & 15, g = l >> 4;
  const u32 cntC = sm + L_MISC, cntW = cntC + 4, cntH = cntC + 8, cntZ = cntC + 12, ltag = cntC + 16;
  const __amdgpu_buffer_rsrc_t rws = __builtin_amdgcn_make_buffer_rsrc(p.ws, 0, (u32)WS_DBG, 0x00020000);
  TS(0); TC(30);

  if (wid < 4) {
    for (int pc = w; pc < 17; pc += 4) {
      int row = r2 + (pc < 9 ? pc - 1 : pc - 9) * 8 + (l >> 3);
      row = row < 0 ? 0 : row;
      int sw = (pc < 9 ? pc : pc - 9) & 3;
      dma16(pc < 9 ? p.k : p.v, row * 1024 + ct * 128 + (((l & 7) ^ (2 * sw)) * 16), sm + L_R + pc * 1024);
    }
    const int cp = l & 7, rg = l >> 3, c0 = ct * 64 + w * 16 + cp * 2;
    u32x4 sk = *(const u32x4*)(p.shift_k + c0 * 8);
    f32x4 acc[6][2];
#pragma unroll
    for (int fb = 0; fb < 6; ++fb) {
      int f = st * 32 + fb * 16 + 4 * g;
      f = f < F_ ? f : 0;
      u32x2 bb = *(const u32x2*)(p.b1 + f * 2);
      f32x4 t = {blo(bb.x), bhi(bb.x), blo(bb.y), bhi(bb.y)};
      acc[fb][0] = t; acc[fb][1] = t;
    }
    if (wid == 0 && l == 0) {
      u64 e = __hip_atomic_load((GAS u64*)(p.ws + WS_EPOCH), __ATOMIC_RELAXED, __HIP_MEMORY_SCOPE_AGENT);
      *(LAS u64*)ltag = e + 1;
      *(LAS u32x4*)cntC = u32x4{0, 0, 0, 0};
    }
    wait_vm<0>();
    bar();
    TS(1);
    {
      const u32 col = (w * 2 + (cp >> 2)) * 16, in = (cp & 3) * 4;
      const u32 a0 = sm + L_R + rg * 1024 + ((col ^ ((rg & 3) * 32)) + in);
      const u32 a1 = sm + L_R + (rg + 1) * 1024 + ((col ^ (((rg + 1) & 3) * 32)) + in);
      u32 kk[11], vv[8];
#pragma unroll
      for (int i = 0; i < 3; ++i) { kk[i] = lds32(a0 + (5 + i) * 128); if (r2 == 0 && rg == 0) kk[i] = 0; }
#pragma unroll
      for (int i = 0; i < 8; ++i) { kk[3 + i] = lds32(a1 + i * 128); vv[i] = lds32(a0 + 9 * 1024 + i * 128); }
      float x0[8], x1[8];
#pragma unroll
      for (int i = 0; i < 8; ++i) {
        x0[i] = (blo(kk[i]) * bhi(sk.y) + blo(kk[i + 1]) * blo(sk.y) + blo(kk[i + 2]) * bhi(sk.x) + blo(kk[i + 3]) * blo(sk.x)) * blo(vv[i]);
        x1[i] = (bhi(kk[i]) * bhi(sk.w) + bhi(kk[i + 1]) * blo(sk.w) + bhi(kk[i + 2]) * bhi(sk.z) + bhi(kk[i + 3]) * blo(sk.z)) * bhi(vv[i]);
      }
      u32x4 o0 = {pk(x0[0], x0[1]), pk(x0[2], x0[3]), pk(x0[4], x0[5]), pk(x0[6], x0[7])};
      u32x4 o1 = {pk(x1[0], x1[1]), pk(x1[2], x1[3]), pk(x1[4], x1[5]), pk(x1[6], x1[7])};
      const u32 off = (u32)WS_KVT + c0 * 4096 + (r2 + rg * 8) * 2;
      __builtin_amdgcn_raw_buffer_store_b128(o0, rws, off, 0, 16);
      __builtin_amdgcn_raw_buffer_store_b128(o1, rws, off + 4096, 0, 16);
    }
    const u64 tag = *(LAS u64*)ltag;
    TS(2);
    const int xr = w * 32 + i16;
    const u32 xa0 = sm + xr * 128 + ((g ^ ((xr >> 1) & 7)) * 16);
    const u32 wa0 = sm + L_W1 + (8 * g + (i16 >> 2)) * 192 + (l & 3) * 8 + (g & 1) * 32;
    const u32 wa1 = wa0 + 32 - (g & 1) * 64;
#pragma unroll 1
    for (int s = 0; s < 8; ++s) {
      if (s == 2) wait_vm<0>();
      bar();
      if (s == 2 && wid == 0 && l == 0) publish(p.ws + WS_FA + (ct * 32 + (r2 >> 6)) * 8, tag);
      TS(16 + s);
      const u32 so = (s & 3) * SLOT;
#pragma unroll
      for (int ks = 0; ks < 2; ++ks) {
        u32x4 xb0 = lds128(((xa0 ^ (ks * 64)) + so)), xb1 = lds128((xa0 ^ (ks * 64)) + so + 2048);
#pragma unroll
        for (int fb = 0; fb < 6; ++fb) {
          const u32 a = ((fb & 1) ? wa1 : wa0) + so + ks * 6144 + (fb >> 1) * 64;
          u32x4 wf = cat(ldstr(a), ldstr(a + 768));
          acc[fb][0] = mfma(wf, xb0, acc[fb][0]);
          acc[fb][1] = mfma(wf, xb1, acc[fb][1]);
        }
      }
    }
    bar();
    TS(3);
#pragma unroll
    for (int u = 0; u < 3; ++u) {
      if (u < nst) {
#pragma unroll
        for (int sb = 0; sb < 2; ++sb) {
          f32x4 a = acc[2 * u][sb], c = acc[2 * u + 1][sb];
          u32x4 o = {pk(gelu(a.x), gelu(a.y)), pk(gelu(a.z), gelu(a.w)), pk(gelu(c.x), gelu(c.y)), pk(gelu(c.z), gelu(c.w))};
          __builtin_amdgcn_raw_buffer_store_b128(o, rws, (u32)WS_H + ((cl * 8 + w * 2 + sb) * NSTEP + st + u) * 1024 + l * 16, 0, 16);
        }
      }
    }
    TS(4);
    wait_vm<0>();
    if (lds_arrive(cntH) == 3 && l == 0) publish(p.ws + WS_FH + (cl * 16 + mem) * 8, tag);
    TS(5);
    lds_wait(cntW, 4);
    const u32 row2 = 4 * g + (i16 >> 2);
    const u32 va = sm + w * SLOT + row2 * 128 + (l & 3) * 8, vsw = ((row2 >> 1) & 3) * 32;
    u32x4 wr[4][4];
#pragma unroll
    for (int i = 0; i < 4; ++i)
#pragma unroll
      for (int nb = 0; nb < 4; ++nb) { const u32 a = va + ((nb * 32) ^ vsw) + i * 4096; wr[i][nb] = cat(ldstr(a), ldstr(a + 2048)); }
#pragma unroll
    for (int i = 0; i < 4; ++i)
#pragma unroll
      for (int nb = 0; nb < 4; ++nb) asm volatile("" : "+v"(wr[i][nb]));
    {
      const u32 vo = (l >> 3) * 1024 + ct * 128 + (((l & 7) ^ (2 * ((l >> 4) & 3))) * 16);
#pragma unroll 1
      for (int i = 0; i < 16; ++i) {
        int row = (w * 11 + 7) * 32 + i * 8;
        row = row < F_ ? row : 0;
        dma16(p.w2 + row * 1024, vo, sm + w * SLOT + i * 1024);
      }
    }
    TS(6);
    poll(p.ws + WS_FH + (cl * 16 + w * 4 + (l & 3)) * 8, tag);
    TS(7);
    f32x4 ac2[4][4];
#pragma unroll
    for (int nb = 0; nb < 4; ++nb)
#pragma unroll
      for (int sb = 0; sb < 4; ++sb) ac2[nb][sb] = f32x4{0, 0, 0, 0};
    const u32 ho = (u32)WS_H + ((cl * 8 + rh * 4) * NSTEP + w * 11) * 1024 + l * 16;
    u32x4 hf[11][4];
    constexpr int WIN = 5;
#pragma unroll
    for (int i = 0; i < WIN; ++i)
#pragma unroll
      for (int sb = 0; sb < 4; ++sb) hf[i][sb] = __builtin_amdgcn_raw_buffer_load_b128(rws, ho + i * 1024, sb * NSTEP * 1024, 16);
#pragma unroll
    for (int i = 0; i < 11; ++i) {
      if (i + WIN < 11) {
        const u32 o = (i + WIN == 10 && w == 3) ? 0x80000000u : ho + (i + WIN) * 1024;
#pragma unroll
        for (int sb = 0; sb < 4; ++sb) hf[i + WIN][sb] = __builtin_amdgcn_raw_buffer_load_b128(rws, o, sb * NSTEP * 1024, 16);
      }
      if (i == 4) wait_vm<24>();
      __builtin_amdgcn_sched_barrier(0);
#pragma unroll
      for (int nb = 0; nb < 4; ++nb) {
        u32x4 wf;
        if (i < 4) wf = wr[i][nb];
        else { const u32 a = va + ((nb * 32) ^ vsw) + (i < 7 ? i : i - 7) * 4096; wf = cat(ldstr(a), ldstr(a + 2048)); }
#pragma unroll
        for (int sb = 0; sb < 4; ++sb) ac2[nb][sb] = mfma(wf, hf[i][sb], ac2[nb][sb]);
      }
    }
    TS(8);
    wait_lgkm0();
#pragma unroll
    for (int nb = 0; nb < 4; ++nb)
#pragma unroll
      for (int sb = 0; sb < 4; ++sb)
        *(LAS f32x4*)(sm + w * SLOT + (sb * 16 + i16) * 256 + (((4 * nb + g) ^ (l & 7)) * 16)) = ac2[nb][sb];
    bar();
    TS(9); TC(31);
    if (b == 0 && wid == 0) {
#pragma unroll 1
      for (int i = 0; i < 4; ++i) poll(p.ws + WS_FA + (i * 64 + l) * 8, tag);
      if (l == 0) publish(p.ws + WS_EPOCH, tag);
    }
  } else {
    const int ch = w >> 1, hf = w & 1, chan = (xcd * 32 + jj) * 2 + ch, tc = w * 64 + l;
    u32 vx[4], vw[3];
#pragma unroll
    for (int i = 0; i < 4; ++i) { int r = (w * 4 + i) * 8 + (l >> 3); vx[i] = (r1 + r) * 1024 + (((l & 7) ^ ((r >> 1) & 7)) * 16); }
#pragma unroll
    for (int i = 0; i < 3; ++i) {
      int idx = (w * 3 + i) * 64 + l, r = idx / 12, c = (idx - r * 12) ^ (2 * ((r >> 3) & 1));
      if (c >= nst * 4) c -= 4;
      vw[i] = r * (F_ * 2) + st * 64 + c * 16;
    }
    auto stage = [&](int s) {
      const u32 so = sm + (s & 3) * SLOT;
#pragma unroll
      for (int i = 0; i < 4; ++i) dma16(p.q + s * 128, vx[i], so + (w * 4 + i) * 1024);
#pragma unroll
      for (int i = 0; i < 3; ++i) dma16(p.w1 + s * (64 * F_ * 2), vw[i], so + L_W1 + (w * 3 + i) * 1024);
    };
    dma16(p.gate_w, l * 16, sm + L_GW);
    stage(0);
    u32x4 o1[2]; u32x2 o2[2];
#pragma unroll
    for (int i = 0; i < 2; ++i) {
      const int a = hf * 64 + l + 128 * i;
      asm volatile("global_load_dwordx4 %0, %1, %2" : "=v"(o1[i]) : "v"(chan * 4096 + (2040 - 8 * a) * 2), "s"(p.ssm_k) : "memory");
      asm volatile("global_load_dwordx2 %0, %1, %2" : "=v"(o2[i]) : "v"(chan * 4096 + (a == 0 ? 0 : (2048 - 8 * a) * 2)), "s"(p.ssm_k) : "memory");
    }
    const float dsk = blo(*(const unsigned short*)(p.ssm_D + chan * 2));
    const u32x4 b2v = *(const u32x4*)(p.b2 + ct * 128 + (l & 7) * 16);
    asm volatile("" ::: "memory");
    stage(1); stage(2); stage(3);
    if (l < 32 && hf == 0) *(LAS u32x4*)(sm + L_U + ch * UCH + l * 16) = u32x4{0, 0, 0, 0};
    bar();
    const u64 tag = *(LAS u64*)ltag;
    const int er = tc >> 3, ec = tc & 7;
    const u32 ga = sm + (rh * 64 + er) * 128 + ((ec ^ ((er >> 1) & 7)) * 16);
    float gs0 = 0.f, gs1 = 0.f;
    u32x4 q0 = {0, 0, 0, 0}, q1 = {0, 0, 0, 0};
    const u32 vo2 = (l >> 3) * 1024 + ct * 128 + (((l & 7) ^ (2 * ((l >> 4) & 3))) * 16);
#pragma unroll 1
    for (int s = 0; s < 9; ++s) {
      if (s == 0) wait_vm<21>(); else wait_vm<14>();
      bar();
      if (s >= 1 && s <= 4) stage(s + 3);
      if (s >= 5) {
        const int fw = (s + 3) & 3;
#pragma unroll
        for (int i = 0; i < 7; ++i) dma16(p.w2 + (fw * 11 * 32 + (w * 7 + i) * 8) * 1024, vo2, sm + fw * SLOT + (w * 7 + i) * 1024);
      }
      if (s < 8) {
        const u32 so = (s & 3) * SLOT;
        u32x4 a0 = lds128(ga + so), a1 = lds128(ga + so + 4096), gw = lds128(sm + L_GW + s * 128 + ec * 16);
#pragma unroll
        for (int i = 0; i < 4; ++i) { gs0 = dot2(a0[i], gw[i], gs0); gs1 = dot2(a1[i], gw[i], gs1); }
        if (s == ct) { q0 = a0; q1 = a1; }
      }
      if (s == 0) {
        asm volatile("" : "+v"(o1[0]), "+v"(o1[1]), "+v"(o2[0]), "+v"(o2[1]));
        const u32 rb = sm + L_R + ch * RCH;
#pragma unroll
        for (int i = 0; i < 3; ++i) {
          u32 d[6];
          if (i < 2) { d[0] = o1[i].x; d[1] = o1[i].y; d[2] = o1[i].z; d[3] = o1[i].w; d[4] = o2[i].x; d[5] = o2[i].y; }
          else { d[0] = d[1] = d[2] = d[3] = 0; d[4] = l == 0 ? o2[0].x : 0; d[5] = l == 0 ? o2[0].y : 0; }
          u32 ro[6], mx[5];
#pragma unroll
          for (int t = 1; t < 6; ++t) { ro[t] = __builtin_amdgcn_alignbit(d[t], d[t], 16); mx[t - 1] = (d[t] & 0xffffu) | (d[t - 1] & 0xffff0000u); }
          const u32 a = rb + (i < 2 ? hf * 64 + l + 128 * i : 256 + l) * 16;
          if (i < 2 || (hf == 0 && l < 2)) {
            *(LAS u32x4*)(a) = u32x4{mx[3], mx[2], mx[1], mx[0]};
            *(LAS u32x4*)(a + RP) = u32x4{ro[4], ro[3], ro[2], ro[1]};
            *(LAS u32x4*)(a + 2 * RP) = u32x4{mx[4], mx[3], mx[2], mx[1]};
            *(LAS u32x4*)(a + 3 * RP) = u32x4{ro[5], ro[4], ro[3], ro[2]};
          }
        }
      }
    }
    TS(1);
    poll(p.ws + WS_FA + (xcd * 32 + (l & 31)) * 8, tag);
    {
      const u32 off = (u32)WS_KVT + chan * 4096 + hf * 2048 + l * 16;
      u32x4 u0 = __builtin_amdgcn_raw_buffer_load_b128(rws, off, 0, 16), u1 = __builtin_amdgcn_raw_buffer_load_b128(rws, off + 1024, 0, 16);
      const u32 a = sm + L_U + ch * UCH + 512 + hf * 2048 + l * 16;
      *(LAS u32x4*)a = u0; *(LAS u32x4*)(a + 1024) = u1;
    }
    wait_vm<0>();
    lds_arrive(cntW);
    lds_wait(cntW, 4);
    TS(2);
    f32x4 y[8];
#pragma unroll
    for (int n = 0; n < 8; ++n) y[n] = f32x4{0, 0, 0, 0};
    {
      u32 ra = sm + L_R + ch * RCH + (i16 & 3) * RP + (2032 - 1792 - 32 * (4 * hf + 3) + 8 * g - (i16 & 12)) * 2;
      u32 rb2 = ra + 8;
      asm volatile("" : "+v"(rb2));
      const u32 ua = sm + L_U + ch * UCH + (256 + 16 * i16 + 8 * g - 16 - 32 * (4 * hf + 3)) * 2;
#pragma unroll
      for (int cc = 0; cc < 4; ++cc) {
        u32x4 A[8], B[8];
#pragma unroll
        for (int n = 0; n < 8; ++n) {
          A[n] = cat(lds64(ra + (7 - n) * 512 + cc * 64), lds64(rb2 + (7 - n) * 512 + cc * 64));
          B[n] = lds128(ua + n * 512 + cc * 64);
        }
#pragma unroll
        for (int n = 0; n < 8; ++n)
#pragma unroll
          for (int pp = 0; pp <= n; ++pp) y[n] = mfma(A[pp], B[n - pp], y[n]);
      }
    }
    TS(3);
    lds_arrive(cntC); lds_wait(cntC, 4);
#pragma unroll
    for (int n = 0; n < 4; ++n) {
      const f32x4 lo = y[n], hi = y[n + 4];
      *(LAS f32x4*)(sm + L_R + (w * 4 + n) * 1024 + l * 16) = sel4(hf, lo, hi);
      y[n] = sel4(hf, hi, lo);
    }
    lds_arrive(cntC); lds_wait(cntC, 8);
#pragma unroll
    for (int n = 0; n < 4; ++n) {
      f32x4 t = *(LAS f32x4*)(sm + L_R + ((w ^ 1) * 4 + n) * 1024 + l * 16) + y[n];
      const int t0 = (hf * 4 + n) * 256 + 16 * i16 + 4 * g;
      u32x2 uu = lds64(sm + L_U + ch * UCH + 512 + t0 * 2);
      u32x2 o = {pk(t.x + blo(uu.x) * dsk, t.y + bhi(uu.x) * dsk), pk(t.z + blo(uu.y) * dsk, t.w + bhi(uu.y) * dsk)};
      __builtin_amdgcn_raw_buffer_store_b64(o, rws, (u32)WS_ZT + chan * 4096 + t0 * 2, 0, 16);
    }
    wait_vm<0>();
    if (lds_arrive(cntZ) == 3 && l == 0) publish(p.ws + WS_FZ + (xcd * 32 + jj) * 8, tag);
    TS(4);
#pragma unroll
    for (int o = 1; o < 8; o <<= 1) { gs0 += __shfl_xor(gs0, o); gs1 += __shfl_xor(gs1, o); }
    const float g0 = __builtin_amdgcn_rcpf(1.0f + __builtin_amdgcn_exp2f(-1.442695041f * gs0));
    const float g1 = __builtin_amdgcn_rcpf(1.0f + __builtin_amdgcn_exp2f(-1.442695041f * gs1));
    poll(p.ws + WS_FZ + (ct * 32 + (l & 31)) * 8, tag);
    TS(5);
    const u32 zb = sm + L_R + 16384;
#pragma unroll
    for (int i = 0; i < 2; ++i) {
      u32x4 z = __builtin_amdgcn_raw_buffer_load_b128(rws, (u32)WS_ZT + (ct * 64 + er + 32 * i) * 4096 + r2 * 2 + ec * 16, 0, 16);
#pragma unroll
      for (int t = 0; t < 4; ++t) *(LAS u32*)(zb + (er + 32 * i) * 132 + ec * 16 + t * 4) = z[t];
    }
    lds_arrive(cntC); lds_wait(cntC, 12);
    float tt[2][8];
#pragma unroll
    for (int i = 0; i < 8; ++i) {
      const u32 a = zb + (ec * 8 + i) * 132 + er * 2;
      const float z0 = blo(*(LAS unsigned short*)a), z1 = blo(*(LAS unsigned short*)(a + 64));
      const float bb = (i & 1) ? bhi(b2v[i >> 1]) : blo(b2v[i >> 1]);
      tt[0][i] = g0 * ((i & 1) ? bhi(q0[i >> 1]) : blo(q0[i >> 1])) * z0 + (1.0f - g0) * bb;
      tt[1][i] = g1 * ((i & 1) ? bhi(q1[i >> 1]) : blo(q1[i >> 1])) * z1 + (1.0f - g1) * bb;
    }
#pragma unroll
    for (int i = 0; i < 8; ++i) asm volatile("" : "+v"(tt[0][i]), "+v"(tt[1][i]));
    TS(6);
    bar();
#pragma unroll
    for (int i = 0; i < 2; ++i) {
      const u32 a = sm + (er + 32 * i) * 256;
      f32x4 s0 = {0, 0, 0, 0}, s1 = {0, 0, 0, 0};
#pragma unroll
      for (int k = 0; k < 4; ++k) {
        s0 += *(LAS f32x4*)(a + k * SLOT + (((2 * ec) ^ (er & 7)) * 16));
        s1 += *(LAS f32x4*)(a + k * SLOT + (((2 * ec + 1) ^ (er & 7)) * 16));
      }
      const float m = 1.0f - (i ? g1 : g0);
      u32x4 o = {pk(tt[i][0] + m * s0.x, tt[i][1] + m * s0.y), pk(tt[i][2] + m * s0.z, tt[i][3] + m * s0.w),
                 pk(tt[i][4] + m * s1.x, tt[i][5] + m * s1.y), pk(tt[i][6] + m * s1.z, tt[i][7] + m * s1.w)};
      *(u32x4*)(p.out + (r2 + er + 32 * i) * 1024 + ct * 128 + ec * 16) = o;
    }
    TS(7);
  }
}

extern "C" void kernel_launch(void* const* d_in, const int* in_sizes, int n_in, void* d_out, int out_size,
                              void* d_ws, size_t ws_size, hipStream_t stream) {
  Params p;
  p.q = (const char*)d_in[0]; p.k = (const char*)d_in[1]; p.v = (const char*)d_in[2];
  p.shift_k = (const char*)d_in[3]; p.ssm_k = (const char*)d_in[4]; p.ssm_D = (const char*)d_in[5];
  p.w1 = (const char*)d_in[7]; p.b1 = (const char*)d_in[8]; p.w2 = (const char*)d_in[9];
  p.b2 = (const char*)d_in[10]; p.gate_w = (const char*)d_in[11];
  p.out = (char*)d_out; p.ws = (char*)d_ws;
  (void)hipFuncSetAttribute((const void*)fused_kernel, hipFuncAttributeMaxDynamicSharedMemorySize, L_TOTAL);
  fused_kernel<<<256, 512, L_TOTAL, stream>>>(p);
}
```

```cpp
#include <hip/hip_runtime.h>
#include <hip/hip_bf16.h>

typedef unsigned u32;
typedef unsigned long long u64;
typedef __attribute__((ext_vector_type(2))) u32 u32x2;
typedef __attribute__((ext_vector_type(4))) u32 u32x4;
typedef __attribute__((ext_vector_type(4))) float f32x4;
typedef __attribute__((ext_vector_type(2))) float f32x2;
typedef __attribute__((ext_vector_type(8))) __bf16 bf16x8;
typedef __attribute__((ext_vector_type(2))) __bf16 bf16x2;
typedef __attribute__((ext_vector_type(4))) short s16x4;
#define LAS __attribute__((address_space(3)))
#define GAS __attribute__((address_space(1)))
#define DEV __device__ __forceinline__

constexpr int S_ = 2048, D_ = 512, F_ = 1376, NSTEP = 43;
constexpr size_t WS_EPOCH = 0, WS_FA = 8192, WS_FH = 16384, WS_FZ = 24576;
constexpr size_t WS_KVT = 65536, WS_ZT = WS_KVT + 2097152, WS_H = WS_ZT + 2097152;
constexpr u32 H_BYTES = 128u * NSTEP * 1024u;
constexpr size_t WS_DBG = WS_H + H_BYTES;
constexpr int SLOT = 28672, L_W1 = 16384;
constexpr int L_R = 4 * SLOT, RP = 4160, RCH = 4 * RP;
constexpr int L_U = L_R + 2 * RCH, UCH = 4608;
constexpr int L_GW = L_U + 2 * UCH;
constexpr int L_GP = L_GW + 1024;
constexpr int L_MISC = L_GP + 1024;
constexpr int L_TOTAL = L_MISC + 64;
constexpr int SPIN_MAX = 1 << 18;
template <int N> struct IC { static constexpr int value = N; };
#define LAG 11
#define PREA 11
#define PUBA 2
#define SMP 4
#define NPRE 8
#define WIN 5
#define WARR 1

#define TSN 0
#define TS(i)
#define TC(i)

struct Params {
  const char *q, *k, *v, *shift_k, *ssm_k, *ssm_D, *w1, *b1, *w2, *b2, *gate_w;
  char *out, *ws;
};

DEV void dma16(const char* sbase, u32 voff, u32 lds) {
  asm volatile("s_mov_b32 m0, %2\n\ts_nop 0\n\tglobal_load_lds_dwordx4 %0, %1" :: "v"(voff), "s"(sbase), "s"(lds) : "memory");
}
template <int N> DEV void wait_vm() { asm volatile("s_waitcnt vmcnt(%0)" :: "n"(N) : "memory"); }
DEV void wait_lgkm0() { asm volatile("s_waitcnt lgkmcnt(0)" ::: "memory"); }
DEV void bar() { asm volatile("s_waitcnt lgkmcnt(0)\n\ts_barrier" ::: "memory"); }
DEV u64 bar_epoch(u64 e) {
  u64 r;
  asm volatile("s_waitcnt lgkmcnt(0)\n\ts_barrier\n\ts_mov_b64 %0, %1" : "=s"(r) : "s"(e) : "memory");
  return r;
}
DEV u32x4 lds128(u32 a) { return *(LAS u32x4*)a; }
DEV u32x2 lds64(u32 a) { return *(LAS u32x2*)a; }
DEV u32 lds32(u32 a) { return *(LAS u32*)a; }
DEV u32x2 ldstr(u32 a) { return __builtin_bit_cast(u32x2, __builtin_amdgcn_ds_read_tr16_b64_v4i16((LAS s16x4*)a)); }
DEV u32x4 cat(u32x2 a, u32x2 b) { u32x4 r = {a.x, a.y, b.x, b.y}; return r; }
DEV f32x4 mfma(u32x4 a, u32x4 b, f32x4 c) {
  return __builtin_amdgcn_mfma_f32_16x16x32_bf16(__builtin_bit_cast(bf16x8, a), __builtin_bit_cast(bf16x8, b), c, 0, 0, 0);
}
DEV f32x4 sel4(int c, f32x4 a, f32x4 b) { f32x4 r = {c ? a.x : b.x, c ? a.y : b.y, c ? a.z : b.z, c ? a.w : b.w}; return r; }
DEV float blo(u32 x) { return __builtin_bit_cast(float, x << 16); }
DEV float bhi(u32 x) { return __builtin_bit_cast(float, x & 0xffff0000u); }
DEV u32 pk(float a, float b) { bf16x2 r = {(__bf16)a, (__bf16)b}; return __builtin_bit_cast(u32, r); }
DEV float dot2(u32 a, u32 b, float c) {
  return __builtin_amdgcn_fdot2_f32_bf16(__builtin_bit_cast(bf16x2, a), __builtin_bit_cast(bf16x2, b), c, false);
}
DEV f32x2 unpk(u32 x) { f32x2 r = {blo(x), bhi(x)}; return r; }
DEV u32 gelu2(float a, float b) {
  const float c1 = -2.302208198f, c2 = -0.1029432397f;
  f32x2 x = {a, b};
  f32x2 u = x * __builtin_elementwise_fma(x * x, f32x2{c2, c2}, f32x2{c1, c1});
  f32x2 d = f32x2{__builtin_amdgcn_exp2f(u.x), __builtin_amdgcn_exp2f(u.y)} + f32x2{1.0f, 1.0f};
  f32x2 y = x * f32x2{__builtin_amdgcn_rcpf(d.x), __builtin_amdgcn_rcpf(d.y)};
  return pk(y.x, y.y);
}
#define SB() __builtin_amdgcn_sched_barrier(0)
DEV void publish(char* flag, u64 tag) { __hip_atomic_store((GAS u64*)flag, tag, __ATOMIC_RELAXED, __HIP_MEMORY_SCOPE_AGENT); }
DEV void poll(const char* flag, u64 tag) {
#pragma unroll 1
  for (int i = 0; i < SPIN_MAX; ++i) {
    u64 x = __hip_atomic_load((GAS u64*)flag, __ATOMIC_RELAXED, __HIP_MEMORY_SCOPE_AGENT);
    if (__all(x == tag)) break;
    __builtin_amdgcn_s_sleep(2);
  }
  asm volatile("" ::: "memory");
}
DEV void lds_arrive(u32 a) {
  if ((threadIdx.x & 63) == 0) asm volatile("ds_add_u32 %0, %1" :: "v"(a), "v"(1u) : "memory");
}
DEV void lds_wait(u32 a, u32 target) {
#pragma unroll 1
  for (int i = 0; i < SPIN_MAX; ++i) {
    u32 x = __hip_atomic_load((LAS u32*)a, __ATOMIC_RELAXED, __HIP_MEMORY_SCOPE_WORKGROUP);
    if (__builtin_amdgcn_readfirstlane(x) >= target) break;
    __builtin_amdgcn_s_sleep(1);
  }
  asm volatile("" ::: "memory");
}

__global__ void __launch_bounds__(512, 2) fused_kernel(Params p) {
  extern __shared__ __attribute__((aligned(16))) char smem[];
  const u32 sm = (u32)(size_t)(LAS char*)smem;
  const int b = blockIdx.x, l = threadIdx.x & 63;
  const int wid = __builtin_amdgcn_readfirstlane(threadIdx.x >> 6), w = wid & 3;
  const int xcd = b & 7, jj = b >> 3, cl = (xcd >> 1) * 4 + (jj >> 3);
  const int rh = xcd & 1, ct = jj & 7, mem = rh * 8 + ct;
  const int ch = w >> 1, hf = w & 1, chan = (xcd * 32 + jj) * 2 + ch;
  const int r1 = cl * 128, r2 = r1 + rh * 64;
  const int st = (mem == 15) ? 41 : (mem >> 2) * 11 + (mem & 3) * 3;
  const int nst = ((mem & 3) == 3 || mem == 14) ? 2 : 3;
  const int i16 = l & 15, g = l >> 4;
  const u32 cntC = sm + L_MISC, cntW = cntC + 4, cntG = cntC + 8;
  const __amdgpu_buffer_rsrc_t rws = __builtin_amdgcn_make_buffer_rsrc(p.ws, 0, (u32)WS_DBG, 0x00020000);
  u64 epoch;
  asm volatile("s_load_dwordx2 %0, %1, 0x0" : "=s"(epoch) : "s"(p.ws) : "memory");
  TS(0); TC(30);

  if (wid < 4) {
    {
      const u32 vo = (r2 + w * 8 + (l >> 3)) * 1024 + ct * 128 + (((l & 7) ^ (2 * w)) * 16);
      dma16(p.k, (r2 == 0 && w == 0) ? vo : vo - 8192, sm + L_R + w * 1024);
      dma16(p.k, vo + 24576, sm + L_R + (w + 4) * 1024);
      if (w == 0) dma16(p.k, vo + 57344, sm + L_R + 8 * 1024);
      dma16(p.v, vo, sm + L_R + (9 + w) * 1024);
      dma16(p.v, vo + 32768, sm + L_R + (13 + w) * 1024);
      if (w == 1) dma16(p.gate_w, l * 16, sm + L_GW);
    }
    const int cp = l & 7, rg = l >> 3, c0 = ct * 64 + w * 16 + cp * 2;
    u32x4 sk = *(const u32x4*)(p.shift_k + c0 * 8);
    u32x2 bb[6];
#pragma unroll
    for (int fb = 0; fb < 6; ++fb) {
      int f = st * 32 + fb * 16 + 4 * g;
      bb[fb] = *(const u32x2*)(p.b1 + (f < F_ ? f : 0) * 2);
    }
    if (wid == 0 && l == 0) *(LAS u32x4*)cntC = u32x4{0, 0, 0, 0};
    wait_vm<0>();
    asm volatile("" : "+v"(sk), "+v"(bb[0]), "+v"(bb[1]), "+v"(bb[2]), "+v"(bb[3]), "+v"(bb[4]), "+v"(bb[5]));
    const u64 tag = bar_epoch(epoch) + 1;
    TS(1);
    {
      const u32 col = (w * 2 + (cp >> 2)) * 16, in = (cp & 3) * 4;
      const u32 a0 = sm + L_R + rg * 1024 + ((col ^ ((rg & 3) * 32)) + in);
      const u32 a1 = sm + L_R + (rg + 1) * 1024 + ((col ^ (((rg + 1) & 3) * 32)) + in);
      u32 kk[11], vv[8];
#pragma unroll
      for (int i = 0; i < 3; ++i) kk[i] = lds32(a0 + (5 + i) * 128);
#pragma unroll
      for (int i = 0; i < 8; ++i) kk[3 + i] = lds32(a1 + i * 128);
#pragma unroll
      for (int i = 0; i < 8; ++i) vv[i] = lds32(a0 + 9 * 1024 + i * 128);
      SB();
      const f32x2 s0 = {blo(sk.x), blo(sk.z)}, s1 = {bhi(sk.x), bhi(sk.z)}, s2 = {blo(sk.y), blo(sk.w)}, s3 = {bhi(sk.y), bhi(sk.w)};
      f32x2 kf[11], x[8];
#pragma unroll
      for (int i = 0; i < 11; ++i) kf[i] = unpk((i < 3 && r2 == 0 && rg == 0) ? 0u : kk[i]);
#pragma unroll
      for (int i = 0; i < 8; ++i)
        x[i] = __builtin_elementwise_fma(kf[i + 3], s0, __builtin_elementwise_fma(kf[i + 2], s1, __builtin_elementwise_fma(kf[i + 1], s2, kf[i] * s3))) * unpk(vv[i]);
      u32x4 o0 = {pk(x[0].x, x[1].x), pk(x[2].x, x[3].x), pk(x[4].x, x[5].x), pk(x[6].x, x[7].x)};
      u32x4 o1 = {pk(x[0].y, x[1].y), pk(x[2].y, x[3].y), pk(x[4].y, x[5].y), pk(x[6].y, x[7].y)};
      const u32 off = (u32)WS_KVT + c0 * 4096 + (r2 + rg * 8) * 2;
      __builtin_amdgcn_raw_buffer_store_b128(o0, rws, off, 0, 16);
      __builtin_amdgcn_raw_buffer_store_b128(o1, rws, off + 4096, 0, 16);
    }
    SB();
    u32x4 o1[2]; u32x2 o2[2];
#pragma unroll
    for (int i = 0; i < 2; ++i) {
      const int a = hf * 64 + l + 128 * i;
      asm volatile("global_load_dwordx4 %0, %1, %2" : "=v"(o1[i]) : "v"(chan * 4096 + (2040 - 8 * a) * 2), "s"(p.ssm_k) : "memory");
      asm volatile("global_load_dwordx2 %0, %1, %2" : "=v"(o2[i]) : "v"(chan * 4096 + (a == 0 ? 0 : (2048 - 8 * a) * 2)), "s"(p.ssm_k) : "memory");
    }
    auto rbuild = [&](int i) {
      u32 d[6];
      if (i < 2) { d[0] = o1[i].x; d[1] = o1[i].y; d[2] = o1[i].z; d[3] = o1[i].w; d[4] = o2[i].x; d[5] = o2[i].y; }
      else { d[0] = d[1] = d[2] = d[3] = 0; d[4] = l == 0 ? o2[0].x : 0; d[5] = l == 0 ? o2[0].y : 0; }
      u32 ro[6], mx[5];
#pragma unroll
      for (int t = 1; t < 6; ++t) { ro[t] = __builtin_amdgcn_alignbit(d[t], d[t], 16); mx[t - 1] = (d[t] & 0xffffu) | (d[t - 1] & 0xffff0000u); }
      const u32 a = sm + L_R + ch * RCH + (i < 2 ? hf * 64 + l + 128 * i : 256 + l) * 16;
      if (i < 2 || (hf == 0 && l < 2)) {
        *(LAS u32x4*)(a) = u32x4{mx[3], mx[2], mx[1], mx[0]};
        *(LAS u32x4*)(a + RP) = u32x4{ro[4], ro[3], ro[2], ro[1]};
        *(LAS u32x4*)(a + 2 * RP) = u32x4{mx[4], mx[3], mx[2], mx[1]};
        *(LAS u32x4*)(a + 3 * RP) = u32x4{ro[5], ro[4], ro[3], ro[2]};
      }
    };
    TS(2);
    f32x4 acc[6][2];
#pragma unroll
    for (int fb = 0; fb < 6; ++fb) { f32x4 t = {blo(bb[fb].x), bhi(bb[fb].x), blo(bb[fb].y), bhi(bb[fb].y)}; acc[fb][0] = t; acc[fb][1] = t; }
    const int xr = w * 32 + i16;
    const u32 xa0 = sm + xr * 128 + ((g ^ ((xr >> 1) & 7)) * 16);
    const u32 wa0 = sm + L_W1 + (8 * g + (i16 >> 2)) * 192 + (l & 3) * 8 + (g & 1) * 32;
    const u32 wa1 = wa0 + 32 - (g & 1) * 64;
    u32x4 wf[17][7], xf[17][2];
    f32x4 ga[2] = {f32x4{0, 0, 0, 0}, f32x4{0, 0, 0, 0}};
    u32 xb[2][2] = {{xa0, xa0 ^ 64}, {xa0 + 2 * SLOT, (xa0 ^ 64) + 2 * SLOT}}, wb[2][2] = {{wa0, wa1}, {wa0 + 2 * SLOT, wa1 + 2 * SLOT}};
    u32 gwa = sm + L_GW + g * 16;
    asm volatile("" : "+v"(xb[0][0]), "+v"(xb[0][1]), "+v"(xb[1][0]), "+v"(xb[1][1]), "+v"(gwa));
    asm volatile("" : "+v"(wb[0][0]), "+v"(wb[0][1]), "+v"(wb[1][0]), "+v"(wb[1][1]));
    auto rd = [&](int k, int i) {
      const int hi = (k >> 2) & 1, so = ((k >> 1) & 1) * SLOT;
      if (i == 0) { xf[k][0] = lds128(xb[hi][k & 1] + so); xf[k][1] = lds128(xb[hi][k & 1] + so + 2048); }
      else if (i < 7) { const u32 a = wb[hi][(i - 1) & 1] + so + (k & 1) * 6144 + ((i - 1) >> 1) * 64; wf[k][i - 1] = cat(ldstr(a), ldstr(a + 768)); }
      else wf[k][6] = lds128(gwa + k * 64);
    };
    bar();
#pragma unroll
    for (int i = 0; i < 8; ++i) rd(0, i);
#pragma unroll
    for (int k = 0; k < 16; ++k) {
#pragma unroll
      for (int i = 0; i < 14; ++i) {
        if (k < 15 && i < 8) rd(k + 1, i);
        SB();
        if (i < 12) acc[i >> 1][i & 1] = mfma(wf[k][i >> 1], xf[k][i & 1], acc[i >> 1][i & 1]);
        else ga[i & 1] = mfma(wf[k][6], xf[k][i & 1], ga[i & 1]);
        SB();
      }
      if (k == 2 * PUBA) {
        wait_vm<0>();
        if (l == 0) publish(p.ws + WS_FA + ((ct * 4 + w) * 32 + (r2 >> 6)) * 8, tag);
      }
      if (k == 2 * PUBA + 2) { asm volatile("" : "+v"(o1[0]), "+v"(o2[0])); rbuild(0); rbuild(2); }
      if (k == 2 * PUBA + 4) { asm volatile("" : "+v"(o1[1]), "+v"(o2[1])); rbuild(1); }
      if (!(k & 1)) bar();
      if (k == 0) TS(16);
    }
    if (ch == rh && l < 16) { *(LAS float*)(sm + L_GP + (hf * 32 + l) * 4) = ga[0].x; *(LAS float*)(sm + L_GP + (hf * 32 + 16 + l) * 4) = ga[1].x; }
    lds_arrive(cntG);
    TS(3);
    __builtin_amdgcn_s_setprio(1);
#pragma unroll
    for (int u = 0; u < 3; ++u) {
      if (u < nst) {
#pragma unroll
        for (int sb = 0; sb < 2; ++sb) {
          f32x4 a = acc[2 * u][sb], c = acc[2 * u + 1][sb];
          u32x4 o = {gelu2(a.x, a.y), gelu2(a.z, a.w), gelu2(c.x, c.y), gelu2(c.z, c.w)};
          __builtin_amdgcn_raw_buffer_store_b128(o, rws, (u32)WS_H + ((cl * 8 + w * 2 + sb) * NSTEP + st + u) * 1024 + l * 16, 0, 16);
          SB();
        }
      }
    }
    TS(4);
    lds_wait(cntW, 4);
    TS(5);
    const u32 row2 = 4 * g + (i16 >> 2);
    const u32 va = sm + w * SLOT + row2 * 128 + (l & 3) * 8, vsw = ((row2 >> 1) & 3) * 32;
    u32x4 wr[11][4];
#pragma unroll
    for (int i = 0; i < 4; ++i)
#pragma unroll
      for (int nb = 0; nb < 4; ++nb) { const u32 a = va + ((nb * 32) ^ vsw) + i * 4096; wr[i][nb] = cat(ldstr(a), ldstr(a + 2048)); }
#pragma unroll
    for (int i = 0; i < 4; ++i)
#pragma unroll
      for (int nb = 0; nb < 4; ++nb) asm volatile("" : "+v"(wr[i][nb]));
    {
      const u32 vo = (l >> 3) * 1024 + ct * 128 + (((l & 7) ^ (2 * ((l >> 4) & 3))) * 16);
#pragma unroll 1
      for (int i = 0; i < 16; ++i) {
        if (i == NPRE) { TS(10); wait_vm<NPRE + TSN>(); if (l == 0) publish(p.ws + WS_FH + (((cl * 2 + (w >> 1)) * 16 + mem) * 2 + (w & 1)) * 8, tag); TS(11); }
        int row = (w * 11 + 7) * 32 + i * 8;
        row = row < F_ ? row : 0;
        dma16(p.w2 + row * 1024, vo, sm + w * SLOT + i * 1024);
      }
    }
    TS(6);
    poll(p.ws + WS_FH + (((cl * 2 + rh) * 16 + w * 4) * 2 + (l & 7)) * 8, tag);
    TS(7);
    f32x4 ac2[4][4];
#pragma unroll
    for (int nb = 0; nb < 4; ++nb)
#pragma unroll
      for (int sb = 0; sb < 4; ++sb) ac2[nb][sb] = f32x4{0, 0, 0, 0};
    const u32 ho = (u32)WS_H + ((cl * 8 + rh * 4) * NSTEP + w * 11) * 1024 + l * 16;
    u32x4 hf[11][4];
#pragma unroll
    for (int i = 0; i < WIN; ++i)
#pragma unroll
      for (int sb = 0; sb < 4; ++sb) hf[i][sb] = __builtin_amdgcn_raw_buffer_load_b128(rws, ho + i * 1024, sb * NSTEP * 1024, 16);
#pragma unroll
    for (int i = 0; i < 11; ++i) {
#pragma unroll
      for (int sb = 0; sb < 4; ++sb) {
        SB();
#pragma unroll
        for (int nb = 0; nb < 4; ++nb) ac2[nb][sb] = mfma(wr[i][nb], hf[i][sb], ac2[nb][sb]);
        SB();
        if (i + WIN < 11) {
          const u32 o = (i + WIN == 10 && w == 3) ? 0x80000000u : ho + (i + WIN) * 1024;
          hf[i + WIN][sb] = __builtin_amdgcn_raw_buffer_load_b128(rws, o, sb * NSTEP * 1024, 16);
        }
        if (i + 2 >= 4 && i + 2 < 11) {
          const u32 a = va + ((sb * 32) ^ vsw) + (i + 2 < 7 ? i + 2 : i + 2 - 7) * 4096;
          wr[i + 2][sb] = cat(ldstr(a), ldstr(a + 2048));
        }
      }
    }
    SB();
    TS(8);
#pragma unroll
    for (int sb = 0; sb < 4; ++sb)
#pragma unroll
      for (int nb = 0; nb < 4; ++nb)
        *(LAS f32x4*)(sm + w * SLOT + (sb * 16 + i16) * 256 + (((4 * nb + g) ^ (l & 7)) * 16)) = ac2[nb][sb];
    bar();
    TS(9); TC(31);
    if (b == 0 && wid == 0) {
#pragma unroll 1
      for (int i = 0; i < 4; ++i) poll(p.ws + WS_FA + ((i * 2 + (l >> 5)) * 128 + (l & 31)) * 8, tag);
      if (l == 0) publish(p.ws + WS_EPOCH, tag);
    }
  } else {
    const int tc = w * 64 + l;
    u32 vx[4], vw[3];
#pragma unroll
    for (int i = 0; i < 4; ++i) { int r = (w * 4 + i) * 8 + (l >> 3); vx[i] = (r1 + r) * 1024 + (((l & 7) ^ ((r >> 1) & 7)) * 16); }
#pragma unroll
    for (int i = 0; i < 3; ++i) {
      int idx = (w * 3 + i) * 64 + l, r = idx / 12, c = (idx - r * 12) ^ (2 * ((r >> 3) & 1));
      if (c >= nst * 4) c -= 4;
      vw[i] = r * (F_ * 2) + st * 64 + c * 16;
    }
    const u32 vo2 = (l >> 3) * 1024 + ct * 128 + (((l & 7) ^ (2 * ((l >> 4) & 3))) * 16);
    auto issue = [&](int from, int to) {
#pragma unroll
      for (int n = from; n < to; ++n) {
        const int s = n / 7, i = n % 7;
        const u32 so = sm + (s & 3) * SLOT;
        if (s >= 8) dma16(p.w2 + ((s & 3) * 11 * 32 + (w * 7 + i) * 8) * 1024, vo2, so + (w * 7 + i) * 1024);
        else if (i < 4) dma16(p.q + s * 128, vx[i], so + (w * 4 + i) * 1024);
        else dma16(p.w1 + s * (64 * F_ * 2), vw[i - 4], so + L_W1 + (w * 3 + i - 4) * 1024);
      }
    };
    issue(0, 7);
    u32 dsk; u32x4 b2v;
    asm volatile("global_load_ushort %0, %1, %2" : "=v"(dsk) : "v"(chan * 2), "s"(p.ssm_D) : "memory");
    asm volatile("global_load_dwordx4 %0, %1, %2" : "=v"(b2v) : "v"(ct * 128 + (l & 7) * 16), "s"(p.b2) : "memory");
    issue(7, PREA);
    if (l < 32 && hf == 0) *(LAS u32x4*)(sm + L_U + ch * UCH + l * 16) = u32x4{0, 0, 0, 0};
    const u64 tag = bar_epoch(epoch) + 1;
    issue(PREA, 21);
    const int er = tc >> 3, ec = tc & 7;
    const u32 ga = sm + (ct & 3) * SLOT + (rh * 64 + er) * 128 + ((ec ^ ((er >> 1) & 7)) * 16);
    u32x4 q0 = {0, 0, 0, 0}, q1 = {0, 0, 0, 0};
    const u32 fao = (u32)WS_FA + ((chan >> 4) * 32 + hf * 16 + i16) * 8, kvo = (u32)WS_KVT + chan * 4096 + hf * 2048 + l * 16;
    const u32 ub = sm + L_U + ch * UCH + 512 + hf * 2048 + l * 16;
    u64 fa = 0; u32x4 u0 = {0, 0, 0, 0}, u1 = {0, 0, 0, 0};
    bool early = false;
#define POS(s) ((s) == 0 ? 21 : (s) == 1 ? 14 + (14 + LAG) / 2 : 7 * (s) + 7 + LAG)
#define EXTRA(s, at, n) (((s) > (at) && POS(at) >= 7 * (s) + 7) ? (n) : 0)
    auto step = [&](auto sc) {
      constexpr int s = sc.value;
      if constexpr (s == SMP + 2) wait_vm<(POS(s) - POS(SMP) < POS(s) - 7 * s - 7 ? POS(s) - POS(SMP) : POS(s) - 7 * s - 7)>();
      else if constexpr (s < 8) wait_vm<POS(s) - 7 * s - 7 + EXTRA(s, SMP, 1) + EXTRA(s, SMP + 2, 2)>();
      bar();
      if (s == ct) { q0 = lds128(ga); q1 = lds128(ga + 4096); }
      if constexpr (s == SMP) asm volatile("global_load_dwordx2 %0, %1, %2 sc1" : "=v"(fa) : "v"(fao), "s"(p.ws) : "memory");
      if constexpr (s == SMP + 2) {
        asm volatile("" : "+v"(fa));
        early = __all(fa == tag);
        asm volatile("global_load_dwordx4 %0, %1, %2 sc1" : "=v"(u0) : "v"(early ? kvo : l * 16), "s"(p.ws) : "memory");
        asm volatile("global_load_dwordx4 %0, %1, %2 offset:1024 sc1" : "=v"(u1) : "v"(early ? kvo : l * 16), "s"(p.ws) : "memory");
      }
      issue(POS(s), s < 8 ? POS(s + 1) : 84);
    };
    step(IC<0>{}); step(IC<1>{}); step(IC<2>{}); step(IC<3>{}); step(IC<4>{}); step(IC<5>{}); step(IC<6>{}); step(IC<7>{}); step(IC<8>{});
    asm volatile("" : "+v"(dsk), "+v"(b2v), "+v"(q0), "+v"(q1));
    TS(1);
    u32 ra = sm + L_R + ch * RCH + (i16 & 3) * RP + (2032 - 1792 - 32 * (4 * hf + 3) + 8 * g - (i16 & 12)) * 2;
    u32 rb2 = ra + 8, ua = sm + L_U + ch * UCH + (256 + 16 * i16 + 8 * g - 16 - 32 * (4 * hf + 3)) * 2;
    asm volatile("" : "+v"(ra), "+v"(rb2), "+v"(ua));
    u32x4 A[4][8], B[4][8];
#pragma unroll
    for (int n = 0; n < 8; ++n) { A[0][n] = cat(lds64(ra + (7 - n) * 512), lds64(rb2 + (7 - n) * 512)); SB(); }
    wait_vm<84 - POS(SMP + 2)>();
    asm volatile("" : "+v"(u0), "+v"(u1));
    if (!early) {
      poll(p.ws + fao, tag);
      u0 = __builtin_amdgcn_raw_buffer_load_b128(rws, kvo, 0, 16); u1 = __builtin_amdgcn_raw_buffer_load_b128(rws, kvo + 1024, 0, 16);
      wait_vm<0>();
      asm volatile("" : "+v"(u0), "+v"(u1));
      TS(10);
    }
    *(LAS u32x4*)ub = u0; *(LAS u32x4*)(ub + 1024) = u1;
    lds_arrive(cntC); lds_wait(cntC, 4);
    TS(2);
    f32x4 y[8];
#pragma unroll
    for (int n = 0; n < 8; ++n) { B[0][n] = lds128(ua + n * 512); y[n] = f32x4{0, 0, 0, 0}; }
#pragma unroll
    for (int cc = 0; cc < 4; ++cc) {
      if (cc == WARR) { wait_vm<0>(); lds_arrive(cntW); }
#pragma unroll
      for (int pp = 0; pp < 8; ++pp)
#pragma unroll
        for (int n = pp; n < 8; ++n) {
          SB();
          y[n] = mfma(A[cc][pp], B[cc][n - pp], y[n]);
          SB();
          const int m = pp * 8 - pp * (pp - 1) / 2 + n - pp - 6;
          if (cc < 3 && m >= 0 && m < 24) {
            const int r = m / 3, o = (cc + 1) * 64;
            if (m % 3 == 0) { u32x2 t = lds64(ra + (7 - r) * 512 + o); A[cc + 1][r].x = t.x; A[cc + 1][r].y = t.y; }
            if (m % 3 == 1) { u32x2 t = lds64(rb2 + (7 - r) * 512 + o); A[cc + 1][r].z = t.x; A[cc + 1][r].w = t.y; }
            if (m % 3 == 2) B[cc + 1][r] = lds128(ua + r * 512 + o);
          }
        }
    }
    SB();
    TS(3);
    lds_arrive(cntC); lds_wait(cntC, 8);
#pragma unroll
    for (int n = 0; n < 4; ++n) {
      const f32x4 lo = y[n], hi = y[n + 4];
      *(LAS f32x4*)(sm + L_R + (w * 4 + n) * 1024 + l * 16) = sel4(hf, lo, hi);
      y[n] = sel4(hf, hi, lo);
    }
    lds_arrive(cntC); lds_wait(cntC, 12);
    {
      u32x2 o[4];
      const float dk = blo(dsk);
#pragma unroll
      for (int n = 0; n < 4; ++n) {
        f32x4 t = *(LAS f32x4*)(sm + L_R + ((w ^ 1) * 4 + n) * 1024 + l * 16) + y[n];
        u32x2 uu = lds64(sm + L_U + ch * UCH + 512 + ((hf * 4 + n) * 256 + 16 * i16 + 4 * g) * 2);
        o[n] = u32x2{pk(t.x + blo(uu.x) * dk, t.y + bhi(uu.x) * dk), pk(t.z + blo(uu.y) * dk, t.w + bhi(uu.y) * dk)};
      }
#pragma unroll
      for (int n = 0; n < 4; n += 2) {
        auto sx = __builtin_amdgcn_permlane16_swap(o[n].x, o[n + 1].x, false, false);
        auto sy = __builtin_amdgcn_permlane16_swap(o[n].y, o[n + 1].y, false, false);
        const int t0 = (hf * 4 + n + (g & 1)) * 256 + 16 * i16 + 8 * (g >> 1);
        __builtin_amdgcn_raw_buffer_store_b128(u32x4{sx[0], sy[0], sx[1], sy[1]}, rws, (u32)WS_ZT + chan * 4096 + t0 * 2, 0, 16);
      }
    }
    TS(11);
    wait_vm<TSN>();
    if (l == 0) publish(p.ws + WS_FZ + (hf * 512 + chan) * 8, tag);
    TS(4);
    lds_wait(cntG, 4);
    const float gs0 = *(LAS float*)(sm + L_GP + er * 4), gs1 = *(LAS float*)(sm + L_GP + 128 + er * 4);
    const float g0 = __builtin_amdgcn_rcpf(1.0f + __builtin_amdgcn_exp2f(-1.442695041f * gs0));
    const float g1 = __builtin_amdgcn_rcpf(1.0f + __builtin_amdgcn_exp2f(-1.442695041f * gs1));
    poll(p.ws + WS_FZ + ((r2 >> 10) * 512 + ct * 64 + (l >> 5) * 32 + w * 8 + (l & 7)) * 8, tag);
    TS(5);
    const u32 zb = sm + L_R + 16384;
#pragma unroll
    for (int i = 0; i < 2; ++i) {
      u32x4 z = __builtin_amdgcn_raw_buffer_load_b128(rws, (u32)WS_ZT + (ct * 64 + er + 32 * i) * 4096 + r2 * 2 + ec * 16, 0, 16);
#pragma unroll
      for (int t = 0; t < 4; ++t) *(LAS u32*)(zb + (er + 32 * i) * 132 + ec * 16 + t * 4) = z[t];
    }
    lds_arrive(cntC); lds_wait(cntC, 16);
    float tt[2][8];
#pragma unroll
    for (int i = 0; i < 8; ++i) {
      const u32 a = zb + (ec * 8 + i) * 132 + er * 2;
      const float z0 = blo(*(LAS unsigned short*)a), z1 = blo(*(LAS unsigned short*)(a + 64));
      const float bb = (i & 1) ? bhi(b2v[i >> 1]) : blo(b2v[i >> 1]);
      tt[0][i] = g0 * ((i & 1) ? bhi(q0[i >> 1]) : blo(q0[i >> 1])) * z0 + (1.0f - g0) * bb;
      tt[1][i] = g1 * ((i & 1) ? bhi(q1[i >> 1]) : blo(q1[i >> 1])) * z1 + (1.0f - g1) * bb;
    }
#pragma unroll
    for (int i = 0; i < 8; ++i) asm volatile("" : "+v"(tt[0][i]), "+v"(tt[1][i]));
    TS(6);
    bar();
#pragma unroll
    for (int i = 0; i < 2; ++i) {
      const u32 a = sm + (er + 32 * i) * 256;
      f32x4 s0 = {0, 0, 0, 0}, s1 = {0, 0, 0, 0};
#pragma unroll
      for (int k = 0; k < 4; ++k) {
        s0 += *(LAS f32x4*)(a + k * SLOT + (((2 * ec) ^ (er & 7)) * 16));
        s1 += *(LAS f32x4*)(a + k * SLOT + (((2 * ec + 1) ^ (er & 7)) * 16));
      }
      const float m = 1.0f - (i ? g1 : g0);
      u32x4 o = {pk(tt[i][0] + m * s0.x, tt[i][1] + m * s0.y), pk(tt[i][2] + m * s0.z, tt[i][3] + m * s0.w),
                 pk(tt[i][4] + m * s1.x, tt[i][5] + m * s1.y), pk(tt[i][6] + m * s1.z, tt[i][7] + m * s1.w)};
      *(u32x4*)(p.out + (r2 + er + 32 * i) * 1024 + ct * 128 + ec * 16) = o;
    }
    TS(7);
  }
}

extern "C" void kernel_launch(void* const* d_in, const int* in_sizes, int n_in, void* d_out, int out_size,
                              void* d_ws, size_t ws_size, hipStream_t stream) {
  Params p;
  p.q = (const char*)d_in[0]; p.k = (const char*)d_in[1]; p.v = (const char*)d_in[2];
  p.shift_k = (const char*)d_in[3]; p.ssm_k = (const char*)d_in[4]; p.ssm_D = (const char*)d_in[5];
  p.w1 = (const char*)d_in[7]; p.b1 = (const char*)d_in[8]; p.w2 = (const char*)d_in[9];
  p.b2 = (const char*)d_in[10]; p.gate_w = (const char*)d_in[11];
  p.out = (char*)d_out; p.ws = (char*)d_ws;
  (void)hipFuncSetAttribute((const void*)fused_kernel, hipFuncAttributeMaxDynamicSharedMemorySize, L_TOTAL);
  fused_kernel<<<256, 512, L_TOTAL, stream>>>(p);
}
```

```cpp
#include <hip/hip_runtime.h>
#include <hip/hip_bf16.h>

typedef unsigned u32;
typedef unsigned long long u64;
typedef __attribute__((ext_vector_type(2))) u32 u32x2;
typedef __attribute__((ext_vector_type(4))) u32 u32x4;
typedef __attribute__((ext_vector_type(4))) float f32x4;
typedef __attribute__((ext_vector_type(2))) float f32x2;
typedef __attribute__((ext_vector_type(8))) __bf16 bf16x8;
typedef __attribute__((ext_vector_type(2))) __bf16 bf16x2;
typedef __attribute__((ext_vector_type(4))) short s16x4;
#define LAS __attribute__((address_space(3)))
#define GAS __attribute__((address_space(1)))
#define DEV __device__ __forceinline__

constexpr int S_ = 2048, D_ = 512, F_ = 1376, NSTEP = 43;
constexpr size_t WS_EPOCH = 0, WS_FA = 8192, WS_FH = 16384, WS_FZ = 24576;
constexpr size_t WS_KVT = 65536, WS_ZT = WS_KVT + 2097152, WS_H = WS_ZT + 2097152;
constexpr u32 H_BYTES = 128u * NSTEP * 1024u;
constexpr size_t WS_DBG = WS_H + H_BYTES;
constexpr int SLOT = 28672, L_W1 = 16384;
constexpr int L_R = 4 * SLOT, RP = 4160, RCH = 4 * RP;
constexpr int L_U = L_R + 2 * RCH, UCH = 4608;
constexpr int L_GW = L_U + 2 * UCH;
constexpr int L_GP = L_GW + 1024;
constexpr int L_MISC = L_GP + 1024;
constexpr int L_TOTAL = L_MISC + 64;
constexpr int SPIN_MAX = 1 << 18;
template <int N> struct IC { static constexpr int value = N; };
#define LAG 11
#define PREA 11
#define PUBA 2
#define SMP 4
#define NPRE -1
#define WIN 12
#define WARR 1

#define HAUX 16
#define PRF 1
#define PRC 0
#define ZTRIG 28
#define ZSL 8
#define HSL 2
#define GARR -1
#define PRZ 2
#define PF 2
#define ROT1 1
#define ROT2 1
#define TS(i)
#define TC(i)
#define TSEND()

struct Params {
  const char *q, *k, *v, *shift_k, *ssm_k, *ssm_D, *w1, *b1, *w2, *b2, *gate_w;
  char *out, *ws;
};

DEV void dma16(const char* sbase, u32 voff, u32 lds) {
  asm volatile("s_mov_b32 m0, %2\n\ts_nop 0\n\tglobal_load_lds_dwordx4 %0, %1" :: "v"(voff), "s"(sbase), "s"(lds) : "memory");
}
template <int N> DEV void wait_vm() { asm volatile("s_waitcnt vmcnt(%0)" :: "n"(N) : "memory"); }
DEV void wait_lgkm0() { asm volatile("s_waitcnt lgkmcnt(0)" ::: "memory"); }
DEV void bar() { asm volatile("s_waitcnt lgkmcnt(0)\n\ts_barrier" ::: "memory"); }
DEV u64 bar_epoch(u64 e) {
  u64 r;
  asm volatile("s_waitcnt lgkmcnt(0)\n\ts_barrier\n\ts_mov_b64 %0, %1" : "=s"(r) : "s"(e) : "memory");
  return r;
}
DEV u32x4 lds128(u32 a) { return *(LAS u32x4*)a; }
DEV u32x2 lds64(u32 a) { return *(LAS u32x2*)a; }
DEV u32 lds32(u32 a) { return *(LAS u32*)a; }
DEV u32x2 ldstr(u32 a) { return __builtin_bit_cast(u32x2, __builtin_amdgcn_ds_read_tr16_b64_v4i16((LAS s16x4*)a)); }
DEV u32x4 cat(u32x2 a, u32x2 b) { u32x4 r = {a.x, a.y, b.x, b.y}; return r; }
DEV f32x4 mfma(u32x4 a, u32x4 b, f32x4 c) {
  return __builtin_amdgcn_mfma_f32_16x16x32_bf16(__builtin_bit_cast(bf16x8, a), __builtin_bit_cast(bf16x8, b), c, 0, 0, 0);
}
DEV f32x4 sel4(int c, f32x4 a, f32x4 b) { f32x4 r = {c ? a.x : b.x, c ? a.y : b.y, c ? a.z : b.z, c ? a.w : b.w}; return r; }
DEV float blo(u32 x) { return __builtin_bit_cast(float, x << 16); }
DEV float bhi(u32 x) { return __builtin_bit_cast(float, x & 0xffff0000u); }
DEV u32 pk(float a, float b) { bf16x2 r = {(__bf16)a, (__bf16)b}; return __builtin_bit_cast(u32, r); }
DEV float dot2(u32 a, u32 b, float c) {
  return __builtin_amdgcn_fdot2_f32_bf16(__builtin_bit_cast(bf16x2, a), __builtin_bit_cast(bf16x2, b), c, false);
}
DEV float add1(float a, float b) { float r = a + b; asm("" : "+v"(r)); return r; }
DEV float mul1(float a, float b) { float r = a * b; asm("" : "+v"(r)); return r; }
DEV float fma1(float a, float b, float c) { float r = __builtin_fmaf(a, b, c); asm("" : "+v"(r)); return r; }
DEV f32x2 unpk(u32 x) { f32x2 r = {blo(x), bhi(x)}; return r; }
DEV u32 gelu2(float a, float b) {
  const float c1 = -2.302208198f, c2 = -0.1029432397f;
  float t0 = a * a, t1 = b * b;
  asm("" : "+v"(t0)); asm("" : "+v"(t1));
  t0 = __builtin_fmaf(t0, c2, c1); t1 = __builtin_fmaf(t1, c2, c1);
  asm("" : "+v"(t0)); asm("" : "+v"(t1));
  t0 = __builtin_amdgcn_exp2f(a * t0); t1 = __builtin_amdgcn_exp2f(b * t1);
  t0 = __builtin_amdgcn_rcpf(1.0f + t0); t1 = __builtin_amdgcn_rcpf(1.0f + t1);
  t0 *= a; asm("" : "+v"(t0)); t1 *= b; asm("" : "+v"(t1));
  return pk(t0, t1);
  f32x2 x = {a, b};
  f32x2 u = x * __builtin_elementwise_fma(x * x, f32x2{c2, c2}, f32x2{c1, c1});
  f32x2 d = f32x2{__builtin_amdgcn_exp2f(u.x), __builtin_amdgcn_exp2f(u.y)} + f32x2{1.0f, 1.0f};
  f32x2 y = x * f32x2{__builtin_amdgcn_rcpf(d.x), __builtin_amdgcn_rcpf(d.y)};
  return pk(y.x, y.y);
}
#define SB() __builtin_amdgcn_sched_barrier(0)
DEV void publish(char* flag, u64 tag) { __hip_atomic_store((GAS u64*)flag, tag, __ATOMIC_RELAXED, __HIP_MEMORY_SCOPE_AGENT); }
template <int SL = 2> DEV void poll(const char* flag, u64 tag) {
#pragma unroll 1
  for (int i = 0; i < SPIN_MAX; ++i) {
    u64 x = __hip_atomic_load((GAS u64*)flag, __ATOMIC_RELAXED, __HIP_MEMORY_SCOPE_AGENT);
    if (__all(x == tag)) break;
    __builtin_amdgcn_s_sleep(SL);
  }
  asm volatile("" ::: "memory");
}
DEV void lds_arrive(u32 a) {
  if ((threadIdx.x & 63) == 0) asm volatile("ds_add_u32 %0, %1" :: "v"(a), "v"(1u) : "memory");
}
DEV void lds_wait(u32 a, u32 target) {
#pragma unroll 1
  for (int i = 0; i < SPIN_MAX; ++i) {
    u32 x = __hip_atomic_load((LAS u32*)a, __ATOMIC_RELAXED, __HIP_MEMORY_SCOPE_WORKGROUP);
    if (__builtin_amdgcn_readfirstlane(x) >= target) break;
    __builtin_amdgcn_s_sleep(1);
  }
  asm volatile("" ::: "memory");
}

__global__ void __launch_bounds__(512, 2) fused_kernel(Params p) {
  extern __shared__ __attribute__((aligned(16))) char smem[];
  const u32 sm = (u32)(size_t)(LAS char*)smem;
  const int b = blockIdx.x, l = threadIdx.x & 63;
  const int wid = __builtin_amdgcn_readfirstlane(threadIdx.x >> 6), w = wid & 3;
  const int xcd = b & 7, jj = b >> 3, cl = (xcd >> 1) * 4 + (jj >> 3);
  const int rh = xcd & 1, ct = jj & 7, mem = rh * 8 + ct;
  const int ch = w >> 1, hf = w & 1, chan = (xcd * 32 + jj) * 2 + ch;
  const int r1 = cl * 128, r2 = r1 + rh * 64;
  const int st = (mem == 15) ? 41 : (mem >> 2) * 11 + (mem & 3) * 3;
  const int nst = ((mem & 3) == 3 || mem == 14) ? 2 : 3;
  const int rot = (ct + 2 * (jj >> 3)) & 7, rot1 = ROT1 ? rot : 0, rot2 = ROT2 ? (rot * 11) >> 3 : 0;
  auto step2 = [&](int i) { const int t = i + rot2; return t >= 11 ? t - 11 : t; };
  const int i16 = l & 15, g = l >> 4;
  const u32 cntC = sm + L_MISC, cntW = cntC + 4, cntG = cntC + 8, cntP = cntC + 16 + ch * 4;
  const __amdgpu_buffer_rsrc_t rws = __builtin_amdgcn_make_buffer_rsrc(p.ws, 0, (u32)WS_DBG, 0x00020000);
  u64 epoch;
  asm volatile("s_load_dwordx2 %0, %1, 0x0" : "=s"(epoch) : "s"(p.ws) : "memory");
  u32 tsv = 0;
  TS(0); TC(30);

  if (wid < 4) {
    {
      const u32 vo = (r2 + w * 8 + (l >> 3)) * 1024 + ct * 128 + (((l & 7) ^ (2 * w)) * 16);
      dma16(p.k, (r2 == 0 && w == 0) ? vo : vo - 8192, sm + L_R + w * 1024);
      dma16(p.k, vo + 24576, sm + L_R + (w + 4) * 1024);
      if (w == 0) dma16(p.k, vo + 57344, sm + L_R + 8 * 1024);
      dma16(p.v, vo, sm + L_R + (9 + w) * 1024);
      dma16(p.v, vo + 32768, sm + L_R + (13 + w) * 1024);
      if (w == 1) dma16(p.gate_w, (l * 16 + rot1 * 128) & 1023, sm + L_GW);
    }
    const int cp = l & 7, rg = l >> 3, c0 = ct * 64 + w * 16 + cp * 2;
    u32x4 sk;
    asm volatile("global_load_dwordx4 %0, %1, %2" : "=v"(sk) : "v"(c0 * 8), "s"(p.shift_k) : "memory");
    u32x2 bb[6];
#pragma unroll
    for (int fb = 0; fb < 6; ++fb) {
      int f = st * 32 + fb * 16 + 4 * g;
      asm volatile("global_load_dwordx2 %0, %1, %2" : "=v"(bb[fb]) : "v"((f < F_ ? f : 0) * 2), "s"(p.b1) : "memory");
    }
    if (wid == 0 && l < 2) *(LAS u32x4*)(cntC + l * 16) = u32x4{0, 0, 0, 0};
    wait_vm<0>();
    asm volatile("" : "+v"(sk), "+v"(bb[0]), "+v"(bb[1]), "+v"(bb[2]), "+v"(bb[3]), "+v"(bb[4]), "+v"(bb[5]));
    const u64 tag = bar_epoch(epoch) + 1;
    TS(1);
    {
      const u32 col = (w * 2 + (cp >> 2)) * 16, in = (cp & 3) * 4;
      const u32 a0 = sm + L_R + rg * 1024 + ((col ^ ((rg & 3) * 32)) + in);
      const u32 a1 = sm + L_R + (rg + 1) * 1024 + ((col ^ (((rg + 1) & 3) * 32)) + in);
      u32 kk[11], vv[8];
#pragma unroll
      for (int i = 0; i < 3; ++i) kk[i] = lds32(a0 + (5 + i) * 128);
#pragma unroll
      for (int i = 0; i < 8; ++i) kk[3 + i] = lds32(a1 + i * 128);
#pragma unroll
      for (int i = 0; i < 8; ++i) vv[i] = lds32(a0 + 9 * 1024 + i * 128);
      SB();
      const f32x2 s0 = {blo(sk.x), blo(sk.z)}, s1 = {bhi(sk.x), bhi(sk.z)}, s2 = {blo(sk.y), blo(sk.w)}, s3 = {bhi(sk.y), bhi(sk.w)};
      f32x2 kf[11], x[8];
#pragma unroll
      for (int i = 0; i < 11; ++i) kf[i] = unpk((i < 3 && r2 == 0 && rg == 0) ? 0u : kk[i]);
#pragma unroll
      for (int i = 0; i < 8; ++i)
        x[i] = __builtin_elementwise_fma(kf[i + 3], s0, __builtin_elementwise_fma(kf[i + 2], s1, __builtin_elementwise_fma(kf[i + 1], s2, kf[i] * s3))) * unpk(vv[i]);
      u32x4 o0 = {pk(x[0].x, x[1].x), pk(x[2].x, x[3].x), pk(x[4].x, x[5].x), pk(x[6].x, x[7].x)};
      u32x4 o1 = {pk(x[0].y, x[1].y), pk(x[2].y, x[3].y), pk(x[4].y, x[5].y), pk(x[6].y, x[7].y)};
      const u32 off = (u32)WS_KVT + c0 * 4096 + (r2 + rg * 8) * 2;
      __builtin_amdgcn_raw_buffer_store_b128(o0, rws, off, 0, 16);
      __builtin_amdgcn_raw_buffer_store_b128(o1, rws, off + 4096, 0, 16);
    }
    SB();
    u32x4 o1[2]; u32x2 o2[2];
#pragma unroll
    for (int i = 0; i < 2; ++i) {
      const int a = hf * 64 + l + 128 * i;
      asm volatile("global_load_dwordx4 %0, %1, %2" : "=v"(o1[i]) : "v"(chan * 4096 + (2040 - 8 * a) * 2), "s"(p.ssm_k) : "memory");
      asm volatile("global_load_dwordx2 %0, %1, %2" : "=v"(o2[i]) : "v"(chan * 4096 + (a == 0 ? 0 : (2048 - 8 * a) * 2)), "s"(p.ssm_k) : "memory");
    }
    auto rbuild = [&](int i) {
      u32 d[6];
      if (i < 2) { d[0] = o1[i].x; d[1] = o1[i].y; d[2] = o1[i].z; d[3] = o1[i].w; d[4] = o2[i].x; d[5] = o2[i].y; }
      else { d[0] = d[1] = d[2] = d[3] = 0; d[4] = l == 0 ? o2[0].x : 0; d[5] = l == 0 ? o2[0].y : 0; }
      u32 ro[6], mx[5];
#pragma unroll
      for (int t = 1; t < 6; ++t) { ro[t] = __builtin_amdgcn_alignbit(d[t], d[t], 16); mx[t - 1] = (d[t] & 0xffffu) | (d[t - 1] & 0xffff0000u); }
      const u32 a = sm + L_R + ch * RCH + (i < 2 ? hf * 64 + l + 128 * i : 256 + l) * 16;
      if (i < 2 || (hf == 0 && l < 2)) {
        *(LAS u32x4*)(a) = u32x4{mx[3], mx[2], mx[1], mx[0]};
        *(LAS u32x4*)(a + RP) = u32x4{ro[4], ro[3], ro[2], ro[1]};
        *(LAS u32x4*)(a + 2 * RP) = u32x4{mx[4], mx[3], mx[2], mx[1]};
        *(LAS u32x4*)(a + 3 * RP) = u32x4{ro[5], ro[4], ro[3], ro[2]};
      }
    };
    { const int ln = b * 43 + w * 11 + (l < 11 - (w == 3) ? l : 0);
      asm volatile("s_mov_b32 m0, %2\n\ts_nop 0\n\tglobal_load_lds_dword %0, %1" :: "v"(ln * 128), "s"(p.w2), "s"(sm + L_GP + 512) : "memory"); }
    TS(2);
    f32x4 acc[6][2];
#pragma unroll
    for (int fb = 0; fb < 6; ++fb) { f32x4 t = {blo(bb[fb].x), bhi(bb[fb].x), blo(bb[fb].y), bhi(bb[fb].y)}; acc[fb][0] = t; acc[fb][1] = t; }
    const int xr = w * 32 + i16;
    const u32 xa0 = sm + xr * 128 + ((g ^ ((xr >> 1) & 7)) * 16);
    const u32 wa0 = sm + L_W1 + (8 * g + (i16 >> 2)) * 192 + (l & 3) * 8 + (g & 1) * 32;
    const u32 wa1 = wa0 + 32 - (g & 1) * 64;
    u32x4 wf[17][7], xf[17][2];
    f32x4 ga[2] = {f32x4{0, 0, 0, 0}, f32x4{0, 0, 0, 0}};
    u32 xb[2][2] = {{xa0, xa0 ^ 64}, {xa0 + 2 * SLOT, (xa0 ^ 64) + 2 * SLOT}}, wb[2][2] = {{wa0, wa1}, {wa0 + 2 * SLOT, wa1 + 2 * SLOT}};
    u32 gwa = sm + L_GW + g * 16;
    asm volatile("" : "+v"(xb[0][0]), "+v"(xb[0][1]), "+v"(xb[1][0]), "+v"(xb[1][1]), "+v"(gwa));
    asm volatile("" : "+v"(wb[0][0]), "+v"(wb[0][1]), "+v"(wb[1][0]), "+v"(wb[1][1]));
    auto rd = [&](int k, int i) {
      const int hi = (k >> 2) & 1, so = ((k >> 1) & 1) * SLOT;
      if (i == 0) { xf[k][0] = lds128(xb[hi][k & 1] + so); xf[k][1] = lds128(xb[hi][k & 1] + so + 2048); }
      else if (i < 7) { const u32 a = wb[hi][(i - 1) & 1] + so + (k & 1) * 6144 + ((i - 1) >> 1) * 64; wf[k][i - 1] = cat(ldstr(a), ldstr(a + 768)); }
      else wf[k][6] = lds128(gwa + k * 64);
    };
    bar();
#pragma unroll
    for (int i = 0; i < 8; ++i) rd(0, i);
#pragma unroll
    for (int k = 0; k < 16; ++k) {
#pragma unroll
      for (int i = 0; i < 14; ++i) {
        if (k < 15 && i < 8) rd(k + 1, i);
        SB();
        if (i < 12) acc[i >> 1][i & 1] = mfma(wf[k][i >> 1], xf[k][i & 1], acc[i >> 1][i & 1]);
        else ga[i & 1] = mfma(wf[k][6], xf[k][i & 1], ga[i & 1]);
        SB();
      }
      if (k == 2 * PUBA) {
        wait_vm<0>();
        if (l == 0) publish(p.ws + WS_FA + ((ct * 4 + w) * 32 + (r2 >> 6)) * 8, tag);
      }
      if (k == 2 * PUBA + 2) { asm volatile("" : "+v"(o1[0]), "+v"(o2[0])); rbuild(0); rbuild(2); }
      if (k == 2 * PUBA + 4) { asm volatile("" : "+v"(o1[1]), "+v"(o2[1])); rbuild(1); }
      if (!(k & 1)) bar();
      if (k == 0) TS(16);
    }
    if (ch == rh && l < 16) { *(LAS float*)(sm + L_GP + (hf * 32 + l) * 4) = ga[0].x; *(LAS float*)(sm + L_GP + (hf * 32 + 16 + l) * 4) = ga[1].x; }
    TS(3);
    if (GARR < 0) lds_arrive(cntG);
    __builtin_amdgcn_s_setprio(PRF);
#pragma unroll
    for (int u = 0; u < 3; ++u) {
      if (u < nst) {
#pragma unroll
        for (int sb = 0; sb < 2; ++sb) {
          f32x4 a = acc[2 * u][sb], c = acc[2 * u + 1][sb];
          u32x4 o = {gelu2(a.x, a.y), gelu2(a.z, a.w), gelu2(c.x, c.y), gelu2(c.z, c.w)};
          __builtin_amdgcn_raw_buffer_store_b128(o, rws, (u32)WS_H + ((cl * 8 + w * 2 + sb) * NSTEP + st + u) * 1024 + l * 16, 0, 16);
          SB();
        }
      }
      if (u == GARR) lds_arrive(cntG);
    }
    TS(4);
    const u32 row2 = 4 * g + (i16 >> 2);
    u32 va[4], pa[4], hv[11];
#pragma unroll
    for (int nb = 0; nb < 4; ++nb) { va[nb] = sm + w * SLOT + row2 * 128 + (l & 3) * 8 + ((nb ^ ((row2 >> 1) & 3)) * 32); pa[nb] = sm + w * SLOT + i16 * 256 + (((4 * nb + g) ^ (l & 7)) * 16); asm volatile("" : "+v"(va[nb]), "+v"(pa[nb])); }
#pragma unroll
    for (int i = 0; i < 11; ++i) {
      const int t = step2(i);
      hv[i] = (u32)WS_H + ((cl * 8 + rh * 4) * NSTEP + w * 11) * 1024 + l * 16 + ((t == 10 && w == 3) ? 0x80000000u : t * 1024u);
      asm volatile("" : "+v"(hv[i]));
    }
    f32x4 ac2[4][4];
#pragma unroll
    for (int i = 0; i < 16; ++i) { ac2[i >> 2][i & 3] = f32x4{0, 0, 0, 0}; asm volatile("" : "+v"(ac2[i >> 2][i & 3])); }
    if (NPRE < 0) { wait_vm<0>(); if (l == 0) publish(p.ws + WS_FH + (((cl * 2 + (w >> 1)) * 16 + mem) * 2 + (w & 1)) * 8, tag); TS(11); }
    lds_wait(cntW, 4);
    TS(5);
    u32x4 wr[11][4];
#pragma unroll
    for (int i = 0; i < 4; ++i)
#pragma unroll
      for (int nb = 0; nb < 4; ++nb) wr[i][nb] = cat(ldstr(va[nb] + i * 4096), ldstr(va[nb] + i * 4096 + 2048));
#pragma unroll
    for (int i = 0; i < 4; ++i)
#pragma unroll
      for (int nb = 0; nb < 4; ++nb) asm volatile("" : "+v"(wr[i][nb]));
    TS(13);
    const u32 vo = (l >> 3) * 1024 + ct * 128 + (((l & 7) ^ (2 * ((l >> 4) & 3))) * 16);
    auto refill = [&](int i) {
      const int row = (w * 11 + step2(7 + (i >> 2))) * 32;
      dma16(p.w2 + ((row < F_ ? row : 0) + (i & 3) * 8) * 1024, vo, sm + w * SLOT + i * 1024);
    };
#pragma unroll
    for (int i = 0; i < 16; ++i) {
      if (i == NPRE) { TS(10); wait_vm<(NPRE < 0 ? 0 : NPRE)>(); if (l == 0) publish(p.ws + WS_FH + (((cl * 2 + (w >> 1)) * 16 + mem) * 2 + (w & 1)) * 8, tag); TS(11); }
      refill(i);
    }
    TS(6);
    poll<HSL>(p.ws + WS_FH + (((cl * 2 + rh) * 16 + w * 4) * 2 + (l & 7)) * 8, tag);
    TS(7);
#define GI(j) ((j) < 32 ? (j) >> 2 : 8 + ((j) - 32) % 3)
#define GS(j) ((j) < 32 ? (j) & 3 : ((j) - 32) / 3)
    u32x4 hf[44];
#pragma unroll
    for (int j = 0; j < WIN; ++j) hf[j] = __builtin_amdgcn_raw_buffer_load_b128(rws, hv[GI(j)], GS(j) * NSTEP * 1024, HAUX);
#pragma unroll
    for (int j = 0; j < 44; ++j) {
      const int i = GI(j), sb = GS(j), jn = j - 1 + WIN;
      if (j == ZTRIG) lds_arrive(cntG);
#pragma unroll
      for (int nb = 0; nb < 4; ++nb) {
        SB();
        asm volatile("v_mfma_f32_16x16x32_bf16 %0, %1, %2, %0" : "+v"(ac2[nb][sb]) : "v"(wr[i][nb]), "v"(hf[j]));
        SB();
        if (nb == 0 && j > 0 && jn < 44) hf[jn] = __builtin_amdgcn_raw_buffer_load_b128(rws, hv[GI(jn)], GS(jn) * NSTEP * 1024, HAUX);
        if ((nb == 1 || nb == 2) && i >= 1 && i < 8) {
          const u32x2 t = ldstr(va[sb] + (i + 3 < 7 ? i + 3 : i + 3 - 7) * 4096 + (nb - 1) * 2048);
          if (nb == 1) { wr[i + 3][sb].x = t.x; wr[i + 3][sb].y = t.y; } else { wr[i + 3][sb].z = t.x; wr[i + 3][sb].w = t.y; }
        }
        const int m = (i - 8) * 4 + nb;
        if (j >= 35 && m % 3 == 1) *(LAS f32x4*)(pa[m / 3] + (sb - 1) * 4096) = ac2[m / 3][sb - 1];
      }
    }
    asm volatile("s_nop 7\n\ts_nop 7" ::: "memory");
    TS(8);
#pragma unroll
    for (int nb = 0; nb < 4; ++nb) *(LAS f32x4*)(pa[nb] + 3 * 4096) = ac2[nb][3];
    bar();
    TS(9); TC(31); TSEND();
    if (b == 0 && wid == 0) {
#pragma unroll 1
      for (int i = 0; i < 4; ++i) poll(p.ws + WS_FA + ((i * 2 + (l >> 5)) * 128 + (l & 31)) * 8, tag);
      if (l == 0) publish(p.ws + WS_EPOCH, tag);
    }
  } else {
    const int tc = w * 64 + l;
    u32 vx[4], vw[3];
#pragma unroll
    for (int i = 0; i < 4; ++i) { int r = (w * 4 + i) * 8 + (l >> 3); vx[i] = (r1 + r) * 1024 + (((l & 7) ^ ((r >> 1) & 7)) * 16); }
#pragma unroll
    for (int i = 0; i < 3; ++i) {
      int idx = (w * 3 + i) * 64 + l, r = idx / 12, c = (idx - r * 12) ^ (2 * ((r >> 3) & 1));
      if (c >= nst * 4) c -= 4;
      vw[i] = r * (F_ * 2) + st * 64 + c * 16;
    }
    const u32 vo2 = (l >> 3) * 1024 + ct * 128 + (((l & 7) ^ (2 * ((l >> 4) & 3))) * 16);
    int r7[7];
#pragma unroll
    for (int i = 0; i < 7; ++i) r7[i] = step2((w * 7 + i) >> 2) * 32 + ((w * 7 + i) & 3) * 8;
    auto issue = [&](int from, int to) {
#pragma unroll
      for (int n = from; n < to; ++n) {
        const int s = n / 7, i = n % 7;
        const u32 so = sm + (s & 3) * SLOT;
        const int sr = (s + rot1) & 7, row = (s & 3) * 11 * 32 + r7[i];
        if (s >= 8) dma16(p.w2 + (s == 11 && row >= F_ ? 0 : row) * 1024, vo2, so + (w * 7 + i) * 1024);
        else if (i < 4) dma16(p.q + sr * 128, vx[i], so + (w * 4 + i) * 1024);
        else dma16(p.w1 + sr * (64 * F_ * 2), vw[i - 4], so + L_W1 + (w * 3 + i - 4) * 1024);
      }
    };
    issue(0, 7);
    u32 dsk; u32x4 b2v;
    asm volatile("global_load_ushort %0, %1, %2" : "=v"(dsk) : "v"(chan * 2), "s"(p.ssm_D) : "memory");
    asm volatile("global_load_dwordx4 %0, %1, %2" : "=v"(b2v) : "v"(ct * 128 + (l & 7) * 16), "s"(p.b2) : "memory");
    issue(7, PREA);
    if (l < 32 && hf == 0) *(LAS u32x4*)(sm + L_U + ch * UCH + l * 16) = u32x4{0, 0, 0, 0};
    const u64 tag = bar_epoch(epoch) + 1;
    issue(PREA, 21);
    const int er = tc >> 3, ec = tc & 7;
    const int sq = (ct - rot1) & 7;
    const u32 ga = sm + (sq & 3) * SLOT + (rh * 64 + er) * 128 + ((ec ^ ((er >> 1) & 7)) * 16);
    u32x4 q0 = {0, 0, 0, 0}, q1 = {0, 0, 0, 0};
    const u32 fao = (u32)WS_FA + ((chan >> 4) * 32 + hf * 16 + i16) * 8, kvo = (u32)WS_KVT + chan * 4096 + hf * 2048 + l * 16;
    const u32 ub = sm + L_U + ch * UCH + 512 + hf * 2048 + l * 16;
    u64 fa = 0; u32x4 u0 = {0, 0, 0, 0}, u1 = {0, 0, 0, 0};
    bool early = false;
#define POS(s) ((s) == 0 ? 21 : (s) == 1 ? 14 + (14 + LAG) / 2 : 7 * (s) + 7 + LAG)
#define EXTRA(s, at, n) (((s) > (at) && POS(at) >= 7 * (s) + 7) ? (n) : 0)
    auto step = [&](auto sc) {
      constexpr int s = sc.value;
      if constexpr (s == SMP + 2) wait_vm<(POS(s) - POS(SMP) < POS(s) - 7 * s - 7 ? POS(s) - POS(SMP) : POS(s) - 7 * s - 7)>();
      else if constexpr (s < 8) wait_vm<POS(s) - 7 * s - 7 + EXTRA(s, SMP, 1) + EXTRA(s, SMP + 2, 2)>();
      bar();
      if (s == sq) { q0 = lds128(ga); q1 = lds128(ga + 4096); }
      if constexpr (s == SMP) asm volatile("global_load_dwordx2 %0, %1, %2 sc1" : "=v"(fa) : "v"(fao), "s"(p.ws) : "memory");
      if constexpr (s == SMP + 2) {
        asm volatile("" : "+v"(fa));
        early = __all(fa == tag);
        asm volatile("global_load_dwordx4 %0, %1, %2 sc1" : "=v"(u0) : "v"(early ? kvo : l * 16), "s"(p.ws) : "memory");
        asm volatile("global_load_dwordx4 %0, %1, %2 offset:1024 sc1" : "=v"(u1) : "v"(early ? kvo : l * 16), "s"(p.ws) : "memory");
      }
      issue(POS(s), s < 8 ? POS(s + 1) : 84);
    };
    step(IC<0>{}); step(IC<1>{}); step(IC<2>{}); step(IC<3>{}); step(IC<4>{}); step(IC<5>{}); step(IC<6>{}); step(IC<7>{}); step(IC<8>{});
    asm volatile("" : "+v"(dsk), "+v"(b2v), "+v"(q0), "+v"(q1));
    __builtin_amdgcn_s_setprio(PRC);
    TS(1);
    u32 ra = sm + L_R + ch * RCH + (i16 & 3) * RP + (2032 - 1792 - 32 * (4 * hf + 3) + 8 * g - (i16 & 12)) * 2;
    u32 rb2 = ra + 8, ua = sm + L_U + ch * UCH + (256 + 16 * i16 + 8 * g - 16 - 32 * (4 * hf + 3)) * 2;
    asm volatile("" : "+v"(ra), "+v"(rb2), "+v"(ua));
    u32x4 A[4][8], B[4][8];
#pragma unroll
    for (int n = 0; n < 8; ++n) { A[0][n] = cat(lds64(ra + (7 - n) * 512), lds64(rb2 + (7 - n) * 512)); SB(); }
    wait_vm<84 - POS(SMP + 2)>();
    asm volatile("" : "+v"(u0), "+v"(u1));
    if (!early) {
      poll(p.ws + fao, tag);
      u0 = __builtin_amdgcn_raw_buffer_load_b128(rws, kvo, 0, 16); u1 = __builtin_amdgcn_raw_buffer_load_b128(rws, kvo + 1024, 0, 16);
      wait_vm<0>();
      asm volatile("" : "+v"(u0), "+v"(u1));
      TS(10);
    }
    *(LAS u32x4*)ub = u0; *(LAS u32x4*)(ub + 1024) = u1;
    lds_arrive(cntP); lds_wait(cntP, 2);
    TS(2);
    f32x4 y[8];
#pragma unroll
    for (int n = 0; n < 8; ++n) { B[0][n] = lds128(ua + n * 512); y[n] = f32x4{0, 0, 0, 0}; }
#pragma unroll
    for (int cc = 0; cc < 4; ++cc) {
      if (cc == WARR) { wait_vm<0>(); lds_arrive(cntW); }
      if (cc == 0) { lds_wait(cntG, 4); TS(12); }
      if (cc == 3) lds_arrive(cntP);
#pragma unroll
      for (int pp = 0; pp < 8; ++pp)
#pragma unroll
        for (int n = pp; n < 8; ++n) {
          SB();
          y[n] = mfma(A[cc][pp], B[cc][n - pp], y[n]);
          SB();
          const int m = pp * 8 - pp * (pp - 1) / 2 + n - pp - 6;
          if (cc < 3 && m >= 0 && m < 24) {
            const int r = m / 3, o = (cc + 1) * 64;
            if (m % 3 == 0) { u32x2 t = lds64(ra + (7 - r) * 512 + o); A[cc + 1][r].x = t.x; A[cc + 1][r].y = t.y; }
            if (m % 3 == 1) { u32x2 t = lds64(rb2 + (7 - r) * 512 + o); A[cc + 1][r].z = t.x; A[cc + 1][r].w = t.y; }
            if (m % 3 == 2) B[cc + 1][r] = lds128(ua + r * 512 + o);
          }
        }
    }
    SB();
    TS(3);
    __builtin_amdgcn_s_setprio(PRZ);
    lds_wait(cntP, 4);
#pragma unroll
    for (int n = 0; n < 4; ++n) {
      const f32x4 lo = y[n], hi = y[n + 4];
      *(LAS f32x4*)(sm + L_R + ch * RCH + (hf * 4 + n) * 1024 + l * 16) = sel4(hf, lo, hi);
      y[n] = sel4(hf, hi, lo);
    }
    lds_arrive(cntP); lds_wait(cntP, 6);
    {
      u32x2 o[4];
      const float dk = blo(dsk);
#pragma unroll
      for (int n = 0; n < 4; ++n) {
        const f32x4 t = *(LAS f32x4*)(sm + L_R + ch * RCH + ((hf ^ 1) * 4 + n) * 1024 + l * 16);
        u32x2 uu = lds64(sm + L_U + ch * UCH + 512 + ((hf * 4 + n) * 256 + 16 * i16 + 4 * g) * 2);
        o[n] = u32x2{pk(add1(t.x, fma1(blo(uu.x), dk, y[n].x)), add1(t.y, fma1(bhi(uu.x), dk, y[n].y))),
                     pk(add1(t.z, fma1(blo(uu.y), dk, y[n].z)), add1(t.w, fma1(bhi(uu.y), dk, y[n].w)))};
      }
#pragma unroll
      for (int n = 0; n < 4; n += 2) {
        auto sx = __builtin_amdgcn_permlane16_swap(o[n].x, o[n + 1].x, false, false);
        auto sy = __builtin_amdgcn_permlane16_swap(o[n].y, o[n + 1].y, false, false);
        const int t0 = (hf * 4 + n + (g & 1)) * 256 + 16 * i16 + 8 * (g >> 1);
        __builtin_amdgcn_raw_buffer_store_b128(u32x4{sx[0], sy[0], sx[1], sy[1]}, rws, (u32)WS_ZT + chan * 4096 + t0 * 2, 0, 16);
      }
    }
    TS(11);
    wait_vm<0>();
    if (l == 0) publish(p.ws + WS_FZ + (hf * 512 + chan) * 8, tag);
    TS(4);
    lds_arrive(cntG); lds_wait(cntG, 12);
    poll<ZSL>(p.ws + WS_FZ + ((r2 >> 10) * 512 + ct * 64 + (l >> 5) * 32 + w * 8 + (l & 7)) * 8, tag);
    TS(5);
    const u32 zb = sm + L_R;
#pragma unroll
    for (int i = 0; i < 2; ++i) {
      u32x4 z = __builtin_amdgcn_raw_buffer_load_b128(rws, (u32)WS_ZT + (ct * 64 + er + 32 * i) * 4096 + r2 * 2 + ec * 16, 0, 16);
#pragma unroll
      for (int t = 0; t < 4; ++t) *(LAS u32*)(zb + (er + 32 * i) * 132 + ec * 16 + t * 4) = z[t];
    }
    const float gs0 = *(LAS float*)(sm + L_GP + er * 4), gs1 = *(LAS float*)(sm + L_GP + 128 + er * 4);
    const float g0 = __builtin_amdgcn_rcpf(1.0f + __builtin_amdgcn_exp2f(-1.442695041f * gs0));
    const float g1 = __builtin_amdgcn_rcpf(1.0f + __builtin_amdgcn_exp2f(-1.442695041f * gs1));
    lds_arrive(cntC); lds_wait(cntC, 4);
    float tt[2][8];
#pragma unroll
    for (int i = 0; i < 8; ++i) {
      const u32 a = zb + (ec * 8 + i) * 132 + er * 2;
      const float z0 = blo(*(LAS unsigned short*)a), z1 = blo(*(LAS unsigned short*)(a + 64));
      const float bb = (i & 1) ? bhi(b2v[i >> 1]) : blo(b2v[i >> 1]);
      tt[0][i] = fma1(mul1(g0, (i & 1) ? bhi(q0[i >> 1]) : blo(q0[i >> 1])), z0, mul1(1.0f - g0, bb));
      tt[1][i] = fma1(mul1(g1, (i & 1) ? bhi(q1[i >> 1]) : blo(q1[i >> 1])), z1, mul1(1.0f - g1, bb));
    }
#pragma unroll
    for (int i = 0; i < 8; ++i) asm volatile("" : "+v"(tt[0][i]), "+v"(tt[1][i]));
    TS(6);
    bar();
#pragma unroll
    for (int i = 0; i < 2; ++i) {
      const u32 a = sm + (er + 32 * i) * 256;
      f32x4 s0 = {0, 0, 0, 0}, s1 = {0, 0, 0, 0};
#pragma unroll
      for (int k = 0; k < 4; ++k) {
        s0 += *(LAS f32x4*)(a + k * SLOT + (((2 * ec) ^ (er & 7)) * 16));
        s1 += *(LAS f32x4*)(a + k * SLOT + (((2 * ec + 1) ^ (er & 7)) * 16));
      }
      const float m = 1.0f - (i ? g1 : g0);
      u32x4 o = {pk(tt[i][0] + m * s0.x, tt[i][1] + m * s0.y), pk(tt[i][2] + m * s0.z, tt[i][3] + m * s0.w),
                 pk(tt[i][4] + m * s1.x, tt[i][5] + m * s1.y), pk(tt[i][6] + m * s1.z, tt[i][7] + m * s1.w)};
      *(u32x4*)(p.out + (r2 + er + 32 * i) * 1024 + ct * 128 + ec * 16) = o;
    }
    TS(7); TSEND();
  }
}

extern "C" void kernel_launch(void* const* d_in, const int* in_sizes, int n_in, void* d_out, int out_size,
                              void* d_ws, size_t ws_size, hipStream_t stream) {
  Params p;
  p.q = (const char*)d_in[0]; p.k = (const char*)d_in[1]; p.v = (const char*)d_in[2];
  p.shift_k = (const char*)d_in[3]; p.ssm_k = (const char*)d_in[4]; p.ssm_D = (const char*)d_in[5];
  p.w1 = (const char*)d_in[7]; p.b1 = (const char*)d_in[8]; p.w2 = (const char*)d_in[9];
  p.b2 = (const char*)d_in[10]; p.gate_w = (const char*)d_in[11];
  p.out = (char*)d_out; p.ws = (char*)d_ws;
  (void)hipFuncSetAttribute((const void*)fused_kernel, hipFuncAttributeMaxDynamicSharedMemorySize, L_TOTAL);
  fused_kernel<<<256, 512, L_TOTAL, stream>>>(p);
}
```

```cpp
#include <hip/hip_runtime.h>
#include <hip/hip_bf16.h>

typedef unsigned u32;
typedef unsigned long long u64;
typedef __attribute__((ext_vector_type(2))) u32 u32x2;
typedef __attribute__((ext_vector_type(4))) u32 u32x4;
typedef __attribute__((ext_vector_type(4))) float f32x4;
typedef __attribute__((ext_vector_type(2))) float f32x2;
typedef __attribute__((ext_vector_type(8))) __bf16 bf16x8;
typedef __attribute__((ext_vector_type(2))) __bf16 bf16x2;
typedef __attribute__((ext_vector_type(4))) short s16x4;
#define LAS __attribute__((address_space(3)))
#define GAS __attribute__((address_space(1)))
#define DEV __device__ __forceinline__

constexpr int S_ = 2048, D_ = 512, F_ = 1376, NSTEP = 43;
constexpr size_t WS_EPOCH = 0, WS_FA = 8192, WS_FH = 16384, WS_FZ = 24576;
constexpr size_t WS_KVT = 65536, WS_ZT = WS_KVT + 2097152, WS_H = WS_ZT + 2097152;
constexpr u32 H_BYTES = 128u * NSTEP * 1024u;
constexpr size_t WS_DBG = WS_H + H_BYTES;
constexpr int SLOT = 28672, L_W1 = 16384;
constexpr int L_R = 4 * SLOT, RP = 4160, RCH = 4 * RP;
constexpr int L_U = L_R + 2 * RCH, UCH = 4608;
constexpr int L_GW = L_U + 2 * UCH;
constexpr int L_GP = L_GW + 1024;
constexpr int L_MISC = L_GP + 1024;
constexpr int L_TOTAL = L_MISC + 64;
constexpr int SPIN_MAX = 1 << 18;
template <int N> struct IC { static constexpr int value = N; };
#define LAG 11
#define PREA 11
#define PUBA 2
#define SMP 4
#define NPRE -1
#define WIN 12
#define WARR 1

#define HAUX 16
#define PRF 1
#define PRC 0
#define ZTRIG 28
#define ZSL 8
#define HSL 2
#define GARR -1
#define PRZ 2
#define PF 2
#define OAUX 16
#define ROT1 1
#define ROT2 1
#define TS(i)
#define TC(i)
#define TSEND()

struct Params {
  const char *q, *k, *v, *shift_k, *ssm_k, *ssm_D, *w1, *b1, *w2, *b2, *gate_w;
  char *out, *ws;
};

DEV void dma16(const char* sbase, u32 voff, u32 lds) {
  asm volatile("s_mov_b32 m0, %2\n\ts_nop 0\n\tglobal_load_lds_dwordx4 %0, %1" :: "v"(voff), "s"(sbase), "s"(lds) : "memory");
}
template <int N> DEV void wait_vm() { asm volatile("s_waitcnt vmcnt(%0)" :: "n"(N) : "memory"); }
DEV void wait_lgkm0() { asm volatile("s_waitcnt lgkmcnt(0)" ::: "memory"); }
DEV void bar() { asm volatile("s_waitcnt lgkmcnt(0)\n\ts_barrier" ::: "memory"); }
DEV u64 bar_epoch(u64 e) {
  u64 r;
  asm volatile("s_waitcnt lgkmcnt(0)\n\ts_barrier\n\ts_mov_b64 %0, %1" : "=s"(r) : "s"(e) : "memory");
  return r;
}
DEV u32x4 lds128(u32 a) { return *(LAS u32x4*)a; }
DEV u32x2 lds64(u32 a) { return *(LAS u32x2*)a; }
DEV u32 lds32(u32 a) { return *(LAS u32*)a; }
DEV u32x2 ldstr(u32 a) { return __builtin_bit_cast(u32x2, __builtin_amdgcn_ds_read_tr16_b64_v4i16((LAS s16x4*)a)); }
DEV u32x4 cat(u32x2 a, u32x2 b) { u32x4 r = {a.x, a.y, b.x, b.y}; return r; }
DEV f32x4 mfma(u32x4 a, u32x4 b, f32x4 c) {
  return __builtin_amdgcn_mfma_f32_16x16x32_bf16(__builtin_bit_cast(bf16x8, a), __builtin_bit_cast(bf16x8, b), c, 0, 0, 0);
}
DEV f32x4 sel4(int c, f32x4 a, f32x4 b) { f32x4 r = {c ? a.x : b.x, c ? a.y : b.y, c ? a.z : b.z, c ? a.w : b.w}; return r; }
DEV float blo(u32 x) { return __builtin_bit_cast(float, x << 16); }
DEV float bhi(u32 x) { return __builtin_bit_cast(float, x & 0xffff0000u); }
DEV u32 pk(float a, float b) { bf16x2 r = {(__bf16)a, (__bf16)b}; return __builtin_bit_cast(u32, r); }
DEV float dot2(u32 a, u32 b, float c) {
  return __builtin_amdgcn_fdot2_f32_bf16(__builtin_bit_cast(bf16x2, a), __builtin_bit_cast(bf16x2, b), c, false);
}
DEV float add1(float a, float b) { float r = a + b; asm("" : "+v"(r)); return r; }
DEV float mul1(float a, float b) { float r = a * b; asm("" : "+v"(r)); return r; }
DEV float fma1(float a, float b, float c) { float r = __builtin_fmaf(a, b, c); asm("" : "+v"(r)); return r; }
DEV f32x2 unpk(u32 x) { f32x2 r = {blo(x), bhi(x)}; return r; }
DEV u32 gelu2(float a, float b) {
  const float c1 = -2.302208198f, c2 = -0.1029432397f;
  float t0 = a * a, t1 = b * b;
  asm("" : "+v"(t0)); asm("" : "+v"(t1));
  t0 = __builtin_fmaf(t0, c2, c1); t1 = __builtin_fmaf(t1, c2, c1);
  asm("" : "+v"(t0)); asm("" : "+v"(t1));
  t0 = __builtin_amdgcn_exp2f(a * t0); t1 = __builtin_amdgcn_exp2f(b * t1);
  t0 = __builtin_amdgcn_rcpf(1.0f + t0); t1 = __builtin_amdgcn_rcpf(1.0f + t1);
  t0 *= a; asm("" : "+v"(t0)); t1 *= b; asm("" : "+v"(t1));
  return pk(t0, t1);
  f32x2 x = {a, b};
  f32x2 u = x * __builtin_elementwise_fma(x * x, f32x2{c2, c2}, f32x2{c1, c1});
  f32x2 d = f32x2{__builtin_amdgcn_exp2f(u.x), __builtin_amdgcn_exp2f(u.y)} + f32x2{1.0f, 1.0f};
  f32x2 y = x * f32x2{__builtin_amdgcn_rcpf(d.x), __builtin_amdgcn_rcpf(d.y)};
  return pk(y.x, y.y);
}
#define SB() __builtin_amdgcn_sched_barrier(0)
DEV void publish(char* flag, u64 tag) { __hip_atomic_store((GAS u64*)flag, tag, __ATOMIC_RELAXED, __HIP_MEMORY_SCOPE_AGENT); }
template <int SL = 2> DEV void poll(const char* flag, u64 tag) {
#pragma unroll 1
  for (int i = 0; i < SPIN_MAX; ++i) {
    u64 x = __hip_atomic_load((GAS u64*)flag, __ATOMIC_RELAXED, __HIP_MEMORY_SCOPE_AGENT);
    if (__all(x == tag)) break;
    __builtin_amdgcn_s_sleep(SL);
  }
  asm volatile("" ::: "memory");
}
DEV void lds_arrive(u32 a) {
  if ((threadIdx.x & 63) == 0) asm volatile("ds_add_u32 %0, %1" :: "v"(a), "v"(1u) : "memory");
}
DEV void lds_wait(u32 a, u32 target) {
#pragma unroll 1
  for (int i = 0; i < SPIN_MAX; ++i) {
    u32 x = __hip_atomic_load((LAS u32*)a, __ATOMIC_RELAXED, __HIP_MEMORY_SCOPE_WORKGROUP);
    if (__builtin_amdgcn_readfirstlane(x) >= target) break;
    __builtin_amdgcn_s_sleep(1);
  }
  asm volatile("" ::: "memory");
}

__global__ void __launch_bounds__(512, 2) fused_kernel(Params p) {
  extern __shared__ __attribute__((aligned(16))) char smem[];
  const u32 sm = (u32)(size_t)(LAS char*)smem;
  const int b = blockIdx.x, l = threadIdx.x & 63;
  const int wid = __builtin_amdgcn_readfirstlane(threadIdx.x >> 6), w = wid & 3;
  const int xcd = b & 7, jj = b >> 3, cl = (xcd >> 1) * 4 + (jj >> 3);
  const int rh = xcd & 1, ct = jj & 7, mem = rh * 8 + ct;
  const int ch = w >> 1, hf = w & 1, chan = (xcd * 32 + jj) * 2 + ch;
  const int r1 = cl * 128, r2 = r1 + rh * 64;
  const int st = (mem == 15) ? 41 : (mem >> 2) * 11 + (mem & 3) * 3;
  const int nst = ((mem & 3) == 3 || mem == 14) ? 2 : 3;
  const int rot = (ct + 2 * (jj >> 3)) & 7, rot1 = ROT1 ? rot : 0, rot2 = ROT2 ? (rot * 11) >> 3 : 0;
  auto step2 = [&](int i) { const int t = i + rot2; return t >= 11 ? t - 11 : t; };
  const int i16 = l & 15, g = l >> 4;
  const u32 cntC = sm + L_MISC, cntW = cntC + 4, cntG = cntC + 8, cntP = cntC + 16 + ch * 4;
  const __amdgpu_buffer_rsrc_t rws = __builtin_amdgcn_make_buffer_rsrc(p.ws, 0, (u32)WS_DBG, 0x00020000);
  u64 epoch;
  asm volatile("s_load_dwordx2 %0, %1, 0x0" : "=s"(epoch) : "s"(p.ws) : "memory");
  u32 tsv = 0;
  TS(0); TC(30);

  if (wid < 4) {
    {
      const u32 vo = (r2 + w * 8 + (l >> 3)) * 1024 + ct * 128 + (((l & 7) ^ (2 * w)) * 16);
      dma16(p.k, (r2 == 0 && w == 0) ? vo : vo - 8192, sm + L_R + w * 1024);
      dma16(p.k, vo + 24576, sm + L_R + (w + 4) * 1024);
      if (w == 0) dma16(p.k, vo + 57344, sm + L_R + 8 * 1024);
      dma16(p.v, vo, sm + L_R + (9 + w) * 1024);
      dma16(p.v, vo + 32768, sm + L_R + (13 + w) * 1024);
      if (w == 1) dma16(p.gate_w, (l * 16 + rot1 * 128) & 1023, sm + L_GW);
    }
    const int cp = l & 7, rg = l >> 3, c0 = ct * 64 + w * 16 + cp * 2;
    u32x4 sk;
    asm volatile("global_load_dwordx4 %0, %1, %2" : "=v"(sk) : "v"(c0 * 8), "s"(p.shift_k) : "memory");
    u32x2 bb[6];
#pragma unroll
    for (int fb = 0; fb < 6; ++fb) {
      int f = st * 32 + fb * 16 + 4 * g;
      asm volatile("global_load_dwordx2 %0, %1, %2" : "=v"(bb[fb]) : "v"((f < F_ ? f : 0) * 2), "s"(p.b1) : "memory");
    }
    if (wid == 0 && l < 2) *(LAS u32x4*)(cntC + l * 16) = u32x4{0, 0, 0, 0};
    wait_vm<0>();
    asm volatile("" : "+v"(sk), "+v"(bb[0]), "+v"(bb[1]), "+v"(bb[2]), "+v"(bb[3]), "+v"(bb[4]), "+v"(bb[5]));
    const u64 tag = bar_epoch(epoch) + 1;
    TS(1);
    {
      const u32 col = (w * 2 + (cp >> 2)) * 16, in = (cp & 3) * 4;
      const u32 a0 = sm + L_R + rg * 1024 + ((col ^ ((rg & 3) * 32)) + in);
      const u32 a1 = sm + L_R + (rg + 1) * 1024 + ((col ^ (((rg + 1) & 3) * 32)) + in);
      u32 kk[11], vv[8];
#pragma unroll
      for (int i = 0; i < 3; ++i) kk[i] = lds32(a0 + (5 + i) * 128);
#pragma unroll
      for (int i = 0; i < 8; ++i) kk[3 + i] = lds32(a1 + i * 128);
#pragma unroll
      for (int i = 0; i < 8; ++i) vv[i] = lds32(a0 + 9 * 1024 + i * 128);
      SB();
      const f32x2 s0 = {blo(sk.x), blo(sk.z)}, s1 = {bhi(sk.x), bhi(sk.z)}, s2 = {blo(sk.y), blo(sk.w)}, s3 = {bhi(sk.y), bhi(sk.w)};
      f32x2 kf[11], x[8];
#pragma unroll
      for (int i = 0; i < 11; ++i) kf[i] = unpk((i < 3 && r2 == 0 && rg == 0) ? 0u : kk[i]);
#pragma unroll
      for (int i = 0; i < 8; ++i)
        x[i] = __builtin_elementwise_fma(kf[i + 3], s0, __builtin_elementwise_fma(kf[i + 2], s1, __builtin_elementwise_fma(kf[i + 1], s2, kf[i] * s3))) * unpk(vv[i]);
      u32x4 o0 = {pk(x[0].x, x[1].x), pk(x[2].x, x[3].x), pk(x[4].x, x[5].x), pk(x[6].x, x[7].x)};
      u32x4 o1 = {pk(x[0].y, x[1].y), pk(x[2].y, x[3].y), pk(x[4].y, x[5].y), pk(x[6].y, x[7].y)};
      const u32 off = (u32)WS_KVT + c0 * 4096 + (r2 + rg * 8) * 2;
      __builtin_amdgcn_raw_buffer_store_b128(o0, rws, off, 0, 16);
      __builtin_amdgcn_raw_buffer_store_b128(o1, rws, off + 4096, 0, 16);
    }
    SB();
    u32x4 o1[2]; u32x2 o2[2];
#pragma unroll
    for (int i = 0; i < 2; ++i) {
      const int a = hf * 64 + l + 128 * i;
      asm volatile("global_load_dwordx4 %0, %1, %2" : "=v"(o1[i]) : "v"(chan * 4096 + (2040 - 8 * a) * 2), "s"(p.ssm_k) : "memory");
      asm volatile("global_load_dwordx2 %0, %1, %2" : "=v"(o2[i]) : "v"(chan * 4096 + (a == 0 ? 0 : (2048 - 8 * a) * 2)), "s"(p.ssm_k) : "memory");
    }
    auto rbuild = [&](int i) {
      u32 d[6];
      if (i < 2) { d[0] = o1[i].x; d[1] = o1[i].y; d[2] = o1[i].z; d[3] = o1[i].w; d[4] = o2[i].x; d[5] = o2[i].y; }
      else { d[0] = d[1] = d[2] = d[3] = 0; d[4] = l == 0 ? o2[0].x : 0; d[5] = l == 0 ? o2[0].y : 0; }
      u32 ro[6], mx[5];
#pragma unroll
      for (int t = 1; t < 6; ++t) { ro[t] = __builtin_amdgcn_alignbit(d[t], d[t], 16); mx[t - 1] = (d[t] & 0xffffu) | (d[t - 1] & 0xffff0000u); }
      const u32 a = sm + L_R + ch * RCH + (i < 2 ? hf * 64 + l + 128 * i : 256 + l) * 16;
      if (i < 2 || (hf == 0 && l < 2)) {
        *(LAS u32x4*)(a) = u32x4{mx[3], mx[2], mx[1], mx[0]};
        *(LAS u32x4*)(a + RP) = u32x4{ro[4], ro[3], ro[2], ro[1]};
        *(LAS u32x4*)(a + 2 * RP) = u32x4{mx[4], mx[3], mx[2], mx[1]};
        *(LAS u32x4*)(a + 3 * RP) = u32x4{ro[5], ro[4], ro[3], ro[2]};
      }
    };
    { const int ln = b * 43 + w * 11 + (l < 11 - (w == 3) ? l : 0);
      asm volatile("s_mov_b32 m0, %2\n\ts_nop 0\n\tglobal_load_lds_dword %0, %1" :: "v"(ln * 128), "s"(p.w2), "s"(sm + L_GP + 512) : "memory"); }
    TS(2);
    f32x4 acc[6][2];
#pragma unroll
    for (int fb = 0; fb < 6; ++fb) { f32x4 t = {blo(bb[fb].x), bhi(bb[fb].x), blo(bb[fb].y), bhi(bb[fb].y)}; acc[fb][0] = t; acc[fb][1] = t; }
    const int xr = w * 32 + i16;
    const u32 xa0 = sm + xr * 128 + ((g ^ ((xr >> 1) & 7)) * 16);
    const u32 wa0 = sm + L_W1 + (8 * g + (i16 >> 2)) * 192 + (l & 3) * 8 + (g & 1) * 32;
    const u32 wa1 = wa0 + 32 - (g & 1) * 64;
    u32x4 wf[17][7], xf[17][2];
    f32x4 ga[2] = {f32x4{0, 0, 0, 0}, f32x4{0, 0, 0, 0}};
    u32 xb[2][2] = {{xa0, xa0 ^ 64}, {xa0 + 2 * SLOT, (xa0 ^ 64) + 2 * SLOT}}, wb[2][2] = {{wa0, wa1}, {wa0 + 2 * SLOT, wa1 + 2 * SLOT}};
    u32 gwa = sm + L_GW + g * 16;
    asm volatile("" : "+v"(xb[0][0]), "+v"(xb[0][1]), "+v"(xb[1][0]), "+v"(xb[1][1]), "+v"(gwa));
    asm volatile("" : "+v"(wb[0][0]), "+v"(wb[0][1]), "+v"(wb[1][0]), "+v"(wb[1][1]));
    auto rd = [&](int k, int i) {
      const int hi = (k >> 2) & 1, so = ((k >> 1) & 1) * SLOT;
      if (i == 0) { xf[k][0] = lds128(xb[hi][k & 1] + so); xf[k][1] = lds128(xb[hi][k & 1] + so + 2048); }
      else if (i < 7) { const u32 a = wb[hi][(i - 1) & 1] + so + (k & 1) * 6144 + ((i - 1) >> 1) * 64; wf[k][i - 1] = cat(ldstr(a), ldstr(a + 768)); }
      else wf[k][6] = lds128(gwa + k * 64);
    };
    bar();
#pragma unroll
    for (int i = 0; i < 8; ++i) rd(0, i);
#pragma unroll
    for (int k = 0; k < 16; ++k) {
#pragma unroll
      for (int i = 0; i < 14; ++i) {
        if (k < 15 && i < 8) rd(k + 1, i);
        SB();
        if (i < 12) acc[i >> 1][i & 1] = mfma(wf[k][i >> 1], xf[k][i & 1], acc[i >> 1][i & 1]);
        else ga[i & 1] = mfma(wf[k][6], xf[k][i & 1], ga[i & 1]);
        SB();
      }
      if (k == 2 * PUBA) {
        wait_vm<0>();
        if (l == 0) publish(p.ws + WS_FA + ((ct * 4 + w) * 32 + (r2 >> 6)) * 8, tag);
      }
      if (k == 2 * PUBA + 2) { asm volatile("" : "+v"(o1[0]), "+v"(o2[0])); rbuild(0); rbuild(2); }
      if (k == 2 * PUBA + 4) { asm volatile("" : "+v"(o1[1]), "+v"(o2[1])); rbuild(1); }
      if (!(k & 1)) bar();
      if (k == 0) TS(16);
    }
    if (ch == rh && l < 16) { *(LAS float*)(sm + L_GP + (hf * 32 + l) * 4) = ga[0].x; *(LAS float*)(sm + L_GP + (hf * 32 + 16 + l) * 4) = ga[1].x; }
    TS(3);
    if (GARR < 0) lds_arrive(cntG);
    __builtin_amdgcn_s_setprio(PRF);
#pragma unroll
    for (int u = 0; u < 3; ++u) {
      if (u < nst) {
#pragma unroll
        for (int sb = 0; sb < 2; ++sb) {
          f32x4 a = acc[2 * u][sb], c = acc[2 * u + 1][sb];
          u32x4 o = {gelu2(a.x, a.y), gelu2(a.z, a.w), gelu2(c.x, c.y), gelu2(c.z, c.w)};
          __builtin_amdgcn_raw_buffer_store_b128(o, rws, (u32)WS_H + ((cl * 8 + w * 2 + sb) * NSTEP + st + u) * 1024 + l * 16, 0, 16);
          SB();
        }
      }
      if (u == GARR) lds_arrive(cntG);
    }
    TS(4);
    const u32 row2 = 4 * g + (i16 >> 2);
    u32 va[4], pa[4], hv[11];
#pragma unroll
    for (int nb = 0; nb < 4; ++nb) { va[nb] = sm + w * SLOT + row2 * 128 + (l & 3) * 8 + ((nb ^ ((row2 >> 1) & 3)) * 32); pa[nb] = sm + w * SLOT + i16 * 256 + (((4 * nb + g) ^ (l & 7)) * 16); asm volatile("" : "+v"(va[nb]), "+v"(pa[nb])); }
#pragma unroll
    for (int i = 0; i < 11; ++i) {
      const int t = step2(i);
      hv[i] = (u32)WS_H + ((cl * 8 + rh * 4) * NSTEP + w * 11) * 1024 + l * 16 + ((t == 10 && w == 3) ? 0x80000000u : t * 1024u);
      asm volatile("" : "+v"(hv[i]));
    }
    f32x4 ac2[4][4];
#pragma unroll
    for (int i = 0; i < 16; ++i) { ac2[i >> 2][i & 3] = f32x4{0, 0, 0, 0}; asm volatile("" : "+v"(ac2[i >> 2][i & 3])); }
    if (b == 0 && wid == 0) {
#pragma unroll 1
      for (int it = 0; it < SPIN_MAX; ++it) {
        bool ok = true;
#pragma unroll
        for (int i = 0; i < 4; ++i)
          ok &= __hip_atomic_load((GAS u64*)(p.ws + WS_FA + ((i * 2 + (l >> 5)) * 128 + (l & 31)) * 8), __ATOMIC_RELAXED, __HIP_MEMORY_SCOPE_AGENT) == tag;
        if (__all(ok)) break;
      }
    }
    if (NPRE < 0) { wait_vm<0>(); if (l == 0) publish(p.ws + WS_FH + (((cl * 2 + (w >> 1)) * 16 + mem) * 2 + (w & 1)) * 8, tag); TS(11); }
    if (b == 0 && threadIdx.x == 0) publish(p.ws + WS_EPOCH, tag);
    lds_wait(cntW, 4);
    TS(5);
    u32x4 wr[11][4];
#pragma unroll
    for (int i = 0; i < 4; ++i)
#pragma unroll
      for (int nb = 0; nb < 4; ++nb) wr[i][nb] = cat(ldstr(va[nb] + i * 4096), ldstr(va[nb] + i * 4096 + 2048));
#pragma unroll
    for (int i = 0; i < 4; ++i)
#pragma unroll
      for (int nb = 0; nb < 4; ++nb) asm volatile("" : "+v"(wr[i][nb]));
    TS(13);
    const u32 vo = (l >> 3) * 1024 + ct * 128 + (((l & 7) ^ (2 * ((l >> 4) & 3))) * 16);
    auto refill = [&](int i) {
      const int row = (w * 11 + step2(7 + (i >> 2))) * 32;
      dma16(p.w2 + ((row < F_ ? row : 0) + (i & 3) * 8) * 1024, vo, sm + w * SLOT + i * 1024);
    };
#pragma unroll
    for (int i = 0; i < 16; ++i) {
      if (i == NPRE) { TS(10); wait_vm<(NPRE < 0 ? 0 : NPRE)>(); if (l == 0) publish(p.ws + WS_FH + (((cl * 2 + (w >> 1)) * 16 + mem) * 2 + (w & 1)) * 8, tag); TS(11); }
      refill(i);
    }
    TS(6);
    poll<HSL>(p.ws + WS_FH + (((cl * 2 + rh) * 16 + w * 4) * 2 + (l & 7)) * 8, tag);
    TS(7);
#define GI(j) ((j) < 32 ? (j) >> 2 : 8 + ((j) - 32) % 3)
#define GS(j) ((j) < 32 ? (j) & 3 : ((j) - 32) / 3)
    u32x4 hf[44];
#pragma unroll
    for (int j = 0; j < WIN; ++j) hf[j] = __builtin_amdgcn_raw_buffer_load_b128(rws, hv[GI(j)], GS(j) * NSTEP * 1024, HAUX);
#pragma unroll
    for (int j = 0; j < 44; ++j) {
      const int i = GI(j), sb = GS(j), jn = j - 1 + WIN;
      if (j == ZTRIG) lds_arrive(cntG);
#pragma unroll
      for (int nb = 0; nb < 4; ++nb) {
        SB();
        asm volatile("v_mfma_f32_16x16x32_bf16 %0, %1, %2, %0" : "+v"(ac2[nb][sb]) : "v"(wr[i][nb]), "v"(hf[j]));
        SB();
        if (nb == 0 && j > 0 && jn < 44) hf[jn] = __builtin_amdgcn_raw_buffer_load_b128(rws, hv[GI(jn)], GS(jn) * NSTEP * 1024, HAUX);
        if ((nb == 1 || nb == 2) && i >= 1 && i < 8) {
          const u32x2 t = ldstr(va[sb] + (i + 3 < 7 ? i + 3 : i + 3 - 7) * 4096 + (nb - 1) * 2048);
          if (nb == 1) { wr[i + 3][sb].x = t.x; wr[i + 3][sb].y = t.y; } else { wr[i + 3][sb].z = t.x; wr[i + 3][sb].w = t.y; }
        }
        const int m = (i - 8) * 4 + nb;
        if (j >= 35 && m % 3 == 1) *(LAS f32x4*)(pa[m / 3] + (sb - 1) * 4096) = ac2[m / 3][sb - 1];
      }
    }
    asm volatile("s_nop 7\n\ts_nop 7" ::: "memory");
    TS(8);
#pragma unroll
    for (int nb = 0; nb < 4; ++nb) *(LAS f32x4*)(pa[nb] + 3 * 4096) = ac2[nb][3];
    bar();
    TS(9); TC(31); TSEND();
  } else {
    const int tc = w * 64 + l;
    u32 vx[4], vw[3];
#pragma unroll
    for (int i = 0; i < 4; ++i) { int r = (w * 4 + i) * 8 + (l >> 3); vx[i] = (r1 + r) * 1024 + (((l & 7) ^ ((r >> 1) & 7)) * 16); }
#pragma unroll
    for (int i = 0; i < 3; ++i) {
      int idx = (w * 3 + i) * 64 + l, r = idx / 12, c = (idx - r * 12) ^ (2 * ((r >> 3) & 1));
      if (c >= nst * 4) c -= 4;
      vw[i] = r * (F_ * 2) + st * 64 + c * 16;
    }
    const u32 vo2 = (l >> 3) * 1024 + ct * 128 + (((l & 7) ^ (2 * ((l >> 4) & 3))) * 16);
    int r7[7];
#pragma unroll
    for (int i = 0; i < 7; ++i) r7[i] = step2((w * 7 + i) >> 2) * 32 + ((w * 7 + i) & 3) * 8;
    auto issue = [&](int from, int to) {
#pragma unroll
      for (int n = from; n < to; ++n) {
        const int s = n / 7, i = n % 7;
        const u32 so = sm + (s & 3) * SLOT;
        const int sr = (s + rot1) & 7, row = (s & 3) * 11 * 32 + r7[i];
        if (s >= 8) dma16(p.w2 + (s == 11 && row >= F_ ? 0 : row) * 1024, vo2, so + (w * 7 + i) * 1024);
        else if (i < 4) dma16(p.q + sr * 128, vx[i], so + (w * 4 + i) * 1024);
        else dma16(p.w1 + sr * (64 * F_ * 2), vw[i - 4], so + L_W1 + (w * 3 + i - 4) * 1024);
      }
    };
    issue(0, 7);
    u32 dsk; u32x4 b2v;
    asm volatile("global_load_ushort %0, %1, %2" : "=v"(dsk) : "v"(chan * 2), "s"(p.ssm_D) : "memory");
    asm volatile("global_load_dwordx4 %0, %1, %2" : "=v"(b2v) : "v"(ct * 128 + (l & 7) * 16), "s"(p.b2) : "memory");
    issue(7, PREA);
    if (l < 32 && hf == 0) *(LAS u32x4*)(sm + L_U + ch * UCH + l * 16) = u32x4{0, 0, 0, 0};
    const u64 tag = bar_epoch(epoch) + 1;
    issue(PREA, 21);
    const int er = tc >> 3, ec = tc & 7;
    const int sq = (ct - rot1) & 7;
    const u32 ga = sm + (sq & 3) * SLOT + (rh * 64 + er) * 128 + ((ec ^ ((er >> 1) & 7)) * 16);
    u32x4 q0 = {0, 0, 0, 0}, q1 = {0, 0, 0, 0};
    const u32 fao = (u32)WS_FA + ((chan >> 4) * 32 + hf * 16 + i16) * 8, kvo = (u32)WS_KVT + chan * 4096 + hf * 2048 + l * 16;
    const u32 ub = sm + L_U + ch * UCH + 512 + hf * 2048 + l * 16;
    u64 fa = 0; u32x4 u0 = {0, 0, 0, 0}, u1 = {0, 0, 0, 0};
    bool early = false;
#define POS(s) ((s) == 0 ? 21 : (s) == 1 ? 14 + (14 + LAG) / 2 : 7 * (s) + 7 + LAG)
#define EXTRA(s, at, n) (((s) > (at) && POS(at) >= 7 * (s) + 7) ? (n) : 0)
    auto step = [&](auto sc) {
      constexpr int s = sc.value;
      if constexpr (s == SMP + 2) wait_vm<(POS(s) - POS(SMP) < POS(s) - 7 * s - 7 ? POS(s) - POS(SMP) : POS(s) - 7 * s - 7)>();
      else if constexpr (s < 8) wait_vm<POS(s) - 7 * s - 7 + EXTRA(s, SMP, 1) + EXTRA(s, SMP + 2, 2)>();
      bar();
      if (s == sq) { q0 = lds128(ga); q1 = lds128(ga + 4096); }
      if constexpr (s == SMP) asm volatile("global_load_dwordx2 %0, %1, %2 sc1" : "=v"(fa) : "v"(fao), "s"(p.ws) : "memory");
      if constexpr (s == SMP + 2) {
        asm volatile("" : "+v"(fa));
        early = __all(fa == tag);
        asm volatile("global_load_dwordx4 %0, %1, %2 sc1" : "=v"(u0) : "v"(early ? kvo : l * 16), "s"(p.ws) : "memory");
        asm volatile("global_load_dwordx4 %0, %1, %2 offset:1024 sc1" : "=v"(u1) : "v"(early ? kvo : l * 16), "s"(p.ws) : "memory");
      }
      issue(POS(s), s < 8 ? POS(s + 1) : 84);
    };
    step(IC<0>{}); step(IC<1>{}); step(IC<2>{}); step(IC<3>{}); step(IC<4>{}); step(IC<5>{}); step(IC<6>{}); step(IC<7>{}); step(IC<8>{});
    asm volatile("" : "+v"(dsk), "+v"(b2v), "+v"(q0), "+v"(q1));
    __builtin_amdgcn_s_setprio(PRC);
    TS(1);
    u32 ra = sm + L_R + ch * RCH + (i16 & 3) * RP + (2032 - 1792 - 32 * (4 * hf + 3) + 8 * g - (i16 & 12)) * 2;
    u32 rb2 = ra + 8, ua = sm + L_U + ch * UCH + (256 + 16 * i16 + 8 * g - 16 - 32 * (4 * hf + 3)) * 2;
    asm volatile("" : "+v"(ra), "+v"(rb2), "+v"(ua));
    u32x4 A[4][8], B[4][8];
#pragma unroll
    for (int n = 0; n < 8; ++n) { A[0][n] = cat(lds64(ra + (7 - n) * 512), lds64(rb2 + (7 - n) * 512)); SB(); }
    wait_vm<84 - POS(SMP + 2)>();
    asm volatile("" : "+v"(u0), "+v"(u1));
    if (!early) {
      poll(p.ws + fao, tag);
      u0 = __builtin_amdgcn_raw_buffer_load_b128(rws, kvo, 0, 16); u1 = __builtin_amdgcn_raw_buffer_load_b128(rws, kvo + 1024, 0, 16);
      wait_vm<0>();
      asm volatile("" : "+v"(u0), "+v"(u1));
      TS(10);
    }
    *(LAS u32x4*)ub = u0; *(LAS u32x4*)(ub + 1024) = u1;
    lds_arrive(cntP); lds_wait(cntP, 2);
    TS(2);
    f32x4 y[8];
#pragma unroll
    for (int n = 0; n < 8; ++n) { B[0][n] = lds128(ua + n * 512); y[n] = f32x4{0, 0, 0, 0}; }
#pragma unroll
    for (int cc = 0; cc < 4; ++cc) {
      if (cc == WARR) { wait_vm<0>(); lds_arrive(cntW); }
      if (cc == 0) { lds_wait(cntG, 4); TS(12); }
      if (cc == 3) lds_arrive(cntP);
#pragma unroll
      for (int pp = 0; pp < 8; ++pp)
#pragma unroll
        for (int n = pp; n < 8; ++n) {
          SB();
          y[n] = mfma(A[cc][pp], B[cc][n - pp], y[n]);
          SB();
          const int m = pp * 8 - pp * (pp - 1) / 2 + n - pp - 6;
          if (cc < 3 && m >= 0 && m < 24) {
            const int r = m / 3, o = (cc + 1) * 64;
            if (m % 3 == 0) { u32x2 t = lds64(ra + (7 - r) * 512 + o); A[cc + 1][r].x = t.x; A[cc + 1][r].y = t.y; }
            if (m % 3 == 1) { u32x2 t = lds64(rb2 + (7 - r) * 512 + o); A[cc + 1][r].z = t.x; A[cc + 1][r].w = t.y; }
            if (m % 3 == 2) B[cc + 1][r] = lds128(ua + r * 512 + o);
          }
        }
    }
    SB();
    TS(3);
    __builtin_amdgcn_s_setprio(PRZ);
    lds_wait(cntP, 4);
#pragma unroll
    for (int n = 0; n < 4; ++n) {
      const f32x4 lo = y[n], hi = y[n + 4];
      *(LAS f32x4*)(sm + L_R + ch * RCH + (hf * 4 + n) * 1024 + l * 16) = sel4(hf, lo, hi);
      y[n] = sel4(hf, hi, lo);
    }
    lds_arrive(cntP); lds_wait(cntP, 6);
    {
      u32x2 o[4];
      const float dk = blo(dsk);
#pragma unroll
      for (int n = 0; n < 4; ++n) {
        const f32x4 t = *(LAS f32x4*)(sm + L_R + ch * RCH + ((hf ^ 1) * 4 + n) * 1024 + l * 16);
        u32x2 uu = lds64(sm + L_U + ch * UCH + 512 + ((hf * 4 + n) * 256 + 16 * i16 + 4 * g) * 2);
        o[n] = u32x2{pk(add1(t.x, fma1(blo(uu.x), dk, y[n].x)), add1(t.y, fma1(bhi(uu.x), dk, y[n].y))),
                     pk(add1(t.z, fma1(blo(uu.y), dk, y[n].z)), add1(t.w, fma1(bhi(uu.y), dk, y[n].w)))};
      }
#pragma unroll
      for (int n = 0; n < 4; n += 2) {
        auto sx = __builtin_amdgcn_permlane16_swap(o[n].x, o[n + 1].x, false, false);
        auto sy = __builtin_amdgcn_permlane16_swap(o[n].y, o[n + 1].y, false, false);
        const int t0 = (hf * 4 + n + (g & 1)) * 256 + 16 * i16 + 8 * (g >> 1);
        __builtin_amdgcn_raw_buffer_store_b128(u32x4{sx[0], sy[0], sx[1], sy[1]}, rws, (u32)WS_ZT + chan * 4096 + t0 * 2, 0, 16);
      }
    }
    TS(11);
    wait_vm<0>();
    if (l == 0) publish(p.ws + WS_FZ + (hf * 512 + chan) * 8, tag);
    TS(4);
    lds_arrive(cntG); lds_wait(cntG, 12);
    poll<ZSL>(p.ws + WS_FZ + ((r2 >> 10) * 512 + ct * 64 + (l >> 5) * 32 + w * 8 + (l & 7)) * 8, tag);
    TS(5);
    const u32 zb = sm + L_R;
#pragma unroll
    for (int i = 0; i < 2; ++i) {
      u32x4 z = __builtin_amdgcn_raw_buffer_load_b128(rws, (u32)WS_ZT + (ct * 64 + er + 32 * i) * 4096 + r2 * 2 + ec * 16, 0, 16);
#pragma unroll
      for (int t = 0; t < 4; ++t) *(LAS u32*)(zb + (er + 32 * i) * 132 + ec * 16 + t * 4) = z[t];
    }
    const float gs0 = *(LAS float*)(sm + L_GP + er * 4), gs1 = *(LAS float*)(sm + L_GP + 128 + er * 4);
    const float g0 = __builtin_amdgcn_rcpf(1.0f + __builtin_amdgcn_exp2f(-1.442695041f * gs0));
    const float g1 = __builtin_amdgcn_rcpf(1.0f + __builtin_amdgcn_exp2f(-1.442695041f * gs1));
    lds_arrive(cntC); lds_wait(cntC, 4);
    float tt[2][8];
#pragma unroll
    for (int i = 0; i < 8; ++i) {
      const u32 a = zb + (ec * 8 + i) * 132 + er * 2;
      const float z0 = blo(*(LAS unsigned short*)a), z1 = blo(*(LAS unsigned short*)(a + 64));
      const float bb = (i & 1) ? bhi(b2v[i >> 1]) : blo(b2v[i >> 1]);
      tt[0][i] = fma1(mul1(g0, (i & 1) ? bhi(q0[i >> 1]) : blo(q0[i >> 1])), z0, mul1(1.0f - g0, bb));
      tt[1][i] = fma1(mul1(g1, (i & 1) ? bhi(q1[i >> 1]) : blo(q1[i >> 1])), z1, mul1(1.0f - g1, bb));
    }
#pragma unroll
    for (int i = 0; i < 8; ++i) asm volatile("" : "+v"(tt[0][i]), "+v"(tt[1][i]));
    TS(6);
    bar();
#pragma unroll
    for (int i = 0; i < 2; ++i) {
      const u32 a = sm + (er + 32 * i) * 256;
      f32x4 s0 = {0, 0, 0, 0}, s1 = {0, 0, 0, 0};
#pragma unroll
      for (int k = 0; k < 4; ++k) {
        s0 += *(LAS f32x4*)(a + k * SLOT + (((2 * ec) ^ (er & 7)) * 16));
        s1 += *(LAS f32x4*)(a + k * SLOT + (((2 * ec + 1) ^ (er & 7)) * 16));
      }
      const float m = 1.0f - (i ? g1 : g0);
      u32x4 o = {pk(tt[i][0] + m * s0.x, tt[i][1] + m * s0.y), pk(tt[i][2] + m * s0.z, tt[i][3] + m * s0.w),
                 pk(tt[i][4] + m * s1.x, tt[i][5] + m * s1.y), pk(tt[i][6] + m * s1.z, tt[i][7] + m * s1.w)};
      __builtin_amdgcn_raw_buffer_store_b128(o, __builtin_amdgcn_make_buffer_rsrc(p.out, 0, S_ * D_ * 2, 0x00020000), (r2 + er + 32 * i) * 1024 + ct * 128 + ec * 16, 0, OAUX);
    }
    TS(7); TSEND();
  }
}

extern "C" void kernel_launch(void* const* d_in, const int* in_sizes, int n_in, void* d_out, int out_size,
                              void* d_ws, size_t ws_size, hipStream_t stream) {
  Params p;
  p.q = (const char*)d_in[0]; p.k = (const char*)d_in[1]; p.v = (const char*)d_in[2];
  p.shift_k = (const char*)d_in[3]; p.ssm_k = (const char*)d_in[4]; p.ssm_D = (const char*)d_in[5];
  p.w1 = (const char*)d_in[7]; p.b1 = (const char*)d_in[8]; p.w2 = (const char*)d_in[9];
  p.b2 = (const char*)d_in[10]; p.gate_w = (const char*)d_in[11];
  p.out = (char*)d_out; p.ws = (char*)d_ws;
  (void)hipFuncSetAttribute((const void*)fused_kernel, hipFuncAttributeMaxDynamicSharedMemorySize, L_TOTAL);
  fused_kernel<<<256, 512, L_TOTAL, stream>>>(p);
}
```

```cpp
#define DLY 7
#define DLYF 0
#include <hip/hip_runtime.h>
#include <hip/hip_bf16.h>

typedef unsigned u32;
typedef unsigned long long u64;
typedef __attribute__((ext_vector_type(2))) u32 u32x2;
typedef __attribute__((ext_vector_type(4))) u32 u32x4;
typedef __attribute__((ext_vector_type(4))) float f32x4;
typedef __attribute__((ext_vector_type(2))) float f32x2;
typedef __attribute__((ext_vector_type(8))) __bf16 bf16x8;
typedef __attribute__((ext_vector_type(2))) __bf16 bf16x2;
typedef __attribute__((ext_vector_type(4))) short s16x4;
#define LAS __attribute__((address_space(3)))
#define GAS __attribute__((address_space(1)))
#define DEV __device__ __forceinline__

constexpr int S_ = 2048, D_ = 512, F_ = 1376, NSTEP = 43;
constexpr size_t WS_EPOCH = 0, WS_FA = 8192, WS_FH = 16384, WS_FZ = 24576;
constexpr size_t WS_KVT = 65536, WS_ZT = WS_KVT + 2097152, WS_H = WS_ZT + 2097152;
constexpr u32 H_BYTES = 128u * NSTEP * 1024u;
constexpr size_t WS_DBG = WS_H + H_BYTES;
constexpr int SLOT = 28672, L_W1 = 16384;
constexpr int L_R = 4 * SLOT, RP = 4160, RCH = 4 * RP;
constexpr int L_U = L_R + 2 * RCH, UCH = 4608;
constexpr int L_GW = L_U + 2 * UCH;
constexpr int L_GP = L_GW + 1024;
constexpr int L_MISC = L_GP + 1024;
constexpr int L_TOTAL = L_MISC + 64;
constexpr int SPIN_MAX = 1 << 18;
template <int N> struct IC { static constexpr int value = N; };
#define LAG 11
#define PREA 11
#define PUBA 2
#define SMP 4
#define NPRE -1
#define WIN 12
#define WARR 1

#define HAUX 16
#define PRF 1
#define PRC 0
#define ZTRIG 28
#define ZSL 8
#define HSL 2
#define GARR -1
#define PRZ 2
#define PF 2
#define OAUX 16
#define ROT1 1
#define ROT2 1
#define TS(i) do { if ((i) == 0) t00 = __builtin_amdgcn_s_memrealtime(); else if ((i) == DLY && (wid < 4) == DLYF) { while (__builtin_amdgcn_s_memrealtime() - t00 < 3000) __builtin_amdgcn_s_sleep(1); } } while (0)
#define TC(i)
#define TSEND()

struct Params {
  const char *q, *k, *v, *shift_k, *ssm_k, *ssm_D, *w1, *b1, *w2, *b2, *gate_w;
  char *out, *ws;
};

DEV void dma16(const char* sbase, u32 voff, u32 lds) {
  asm volatile("s_mov_b32 m0, %2\n\ts_nop 0\n\tglobal_load_lds_dwordx4 %0, %1" :: "v"(voff), "s"(sbase), "s"(lds) : "memory");
}
template <int N> DEV void wait_vm() { asm volatile("s_waitcnt vmcnt(%0)" :: "n"(N) : "memory"); }
DEV void wait_lgkm0() { asm volatile("s_waitcnt lgkmcnt(0)" ::: "memory"); }
DEV void bar() { asm volatile("s_waitcnt lgkmcnt(0)\n\ts_barrier" ::: "memory"); }
DEV u64 bar_epoch(u64 e) {
  u64 r;
  asm volatile("s_waitcnt lgkmcnt(0)\n\ts_barrier\n\ts_mov_b64 %0, %1" : "=s"(r) : "s"(e) : "memory");
  return r;
}
DEV u32x4 lds128(u32 a) { return *(LAS u32x4*)a; }
DEV u32x2 lds64(u32 a) { return *(LAS u32x2*)a; }
DEV u32 lds32(u32 a) { return *(LAS u32*)a; }
DEV u32x2 ldstr(u32 a) { return __builtin_bit_cast(u32x2, __builtin_amdgcn_ds_read_tr16_b64_v4i16((LAS s16x4*)a)); }
DEV u32x4 cat(u32x2 a, u32x2 b) { u32x4 r = {a.x, a.y, b.x, b.y}; return r; }
DEV f32x4 mfma(u32x4 a, u32x4 b, f32x4 c) {
  return __builtin_amdgcn_mfma_f32_16x16x32_bf16(__builtin_bit_cast(bf16x8, a), __builtin_bit_cast(bf16x8, b), c, 0, 0, 0);
}
DEV f32x4 sel4(int c, f32x4 a, f32x4 b) { f32x4 r = {c ? a.x : b.x, c ? a.y : b.y, c ? a.z : b.z, c ? a.w : b.w}; return r; }
DEV float blo(u32 x) { return __builtin_bit_cast(float, x << 16); }
DEV float bhi(u32 x) { return __builtin_bit_cast(float, x & 0xffff0000u); }
DEV u32 pk(float a, float b) { bf16x2 r = {(__bf16)a, (__bf16)b}; return __builtin_bit_cast(u32, r); }
DEV float dot2(u32 a, u32 b, float c) {
  return __builtin_amdgcn_fdot2_f32_bf16(__builtin_bit_cast(bf16x2, a), __builtin_bit_cast(bf16x2, b), c, false);
}
DEV float add1(float a, float b) { float r = a + b; asm("" : "+v"(r)); return r; }
DEV float mul1(float a, float b) { float r = a * b; asm("" : "+v"(r)); return r; }
DEV float fma1(float a, float b, float c) { float r = __builtin_fmaf(a, b, c); asm("" : "+v"(r)); return r; }
DEV f32x2 unpk(u32 x) { f32x2 r = {blo(x), bhi(x)}; return r; }
DEV u32 gelu2(float a, float b) {
  const float c1 = -2.302208198f, c2 = -0.1029432397f;
  float t0 = a * a, t1 = b * b;
  asm("" : "+v"(t0)); asm("" : "+v"(t1));
  t0 = __builtin_fmaf(t0, c2, c1); t1 = __builtin_fmaf(t1, c2, c1);
  asm("" : "+v"(t0)); asm("" : "+v"(t1));
  t0 = __builtin_amdgcn_exp2f(a * t0); t1 = __builtin_amdgcn_exp2f(b * t1);
  t0 = __builtin_amdgcn_rcpf(1.0f + t0); t1 = __builtin_amdgcn_rcpf(1.0f + t1);
  t0 *= a; asm("" : "+v"(t0)); t1 *= b; asm("" : "+v"(t1));
  return pk(t0, t1);
  f32x2 x = {a, b};
  f32x2 u = x * __builtin_elementwise_fma(x * x, f32x2{c2, c2}, f32x2{c1, c1});
  f32x2 d = f32x2{__builtin_amdgcn_exp2f(u.x), __builtin_amdgcn_exp2f(u.y)} + f32x2{1.0f, 1.0f};
  f32x2 y = x * f32x2{__builtin_amdgcn_rcpf(d.x), __builtin_amdgcn_rcpf(d.y)};
  return pk(y.x, y.y);
}
#define SB() __builtin_amdgcn_sched_barrier(0)
DEV void publish(char* flag, u64 tag) { __hip_atomic_store((GAS u64*)flag, tag, __ATOMIC_RELAXED, __HIP_MEMORY_SCOPE_AGENT); }
template <int SL = 2> DEV void poll(const char* flag, u64 tag) {
#pragma unroll 1
  for (int i = 0; i < SPIN_MAX; ++i) {
    u64 x = __hip_atomic_load((GAS u64*)flag, __ATOMIC_RELAXED, __HIP_MEMORY_SCOPE_AGENT);
    if (__all(x == tag)) break;
    __builtin_amdgcn_s_sleep(SL);
  }
  asm volatile("" ::: "memory");
}
DEV void lds_arrive(u32 a) {
  if ((threadIdx.x & 63) == 0) asm volatile("ds_add_u32 %0, %1" :: "v"(a), "v"(1u) : "memory");
}
DEV void lds_wait(u32 a, u32 target) {
#pragma unroll 1
  for (int i = 0; i < SPIN_MAX; ++i) {
    u32 x = __hip_atomic_load((LAS u32*)a, __ATOMIC_RELAXED, __HIP_MEMORY_SCOPE_WORKGROUP);
    if (__builtin_amdgcn_readfirstlane(x) >= target) break;
    __builtin_amdgcn_s_sleep(1);
  }
  asm volatile("" ::: "memory");
}

__global__ void __launch_bounds__(512, 2) fused_kernel(Params p) {
  extern __shared__ __attribute__((aligned(16))) char smem[];
  const u32 sm = (u32)(size_t)(LAS char*)smem;
  const int b = blockIdx.x, l = threadIdx.x & 63;
  const int wid = __builtin_amdgcn_readfirstlane(threadIdx.x >> 6), w = wid & 3;
  const int xcd = b & 7, jj = b >> 3, cl = (xcd >> 1) * 4 + (jj >> 3);
  const int rh = xcd & 1, ct = jj & 7, mem = rh * 8 + ct;
  const int ch = w >> 1, hf = w & 1, chan = (xcd * 32 + jj) * 2 + ch;
  const int r1 = cl * 128, r2 = r1 + rh * 64;
  const int st = (mem == 15) ? 41 : (mem >> 2) * 11 + (mem & 3) * 3;
  const int nst = ((mem & 3) == 3 || mem == 14) ? 2 : 3;
  const int rot = (ct + 2 * (jj >> 3)) & 7, rot1 = ROT1 ? rot : 0, rot2 = ROT2 ? (rot * 11) >> 3 : 0;
  auto step2 = [&](int i) { const int t = i + rot2; return t >= 11 ? t - 11 : t; };
  const int i16 = l & 15, g = l >> 4;
  const u32 cntC = sm + L_MISC, cntW = cntC + 4, cntG = cntC + 8, cntP = cntC + 16 + ch * 4;
  const __amdgpu_buffer_rsrc_t rws = __builtin_amdgcn_make_buffer_rsrc(p.ws, 0, (u32)WS_DBG, 0x00020000);
  u64 epoch;
  asm volatile("s_load_dwordx2 %0, %1, 0x0" : "=s"(epoch) : "s"(p.ws) : "memory");
  u32 tsv = 0; u64 t00;
  TS(0); TC(30);

  if (wid < 4) {
    {
      const u32 vo = (r2 + w * 8 + (l >> 3)) * 1024 + ct * 128 + (((l & 7) ^ (2 * w)) * 16);
      dma16(p.k, (r2 == 0 && w == 0) ? vo : vo - 8192, sm + L_R + w * 1024);
      dma16(p.k, vo + 24576, sm + L_R + (w + 4) * 1024);
      if (w == 0) dma16(p.k, vo + 57344, sm + L_R + 8 * 1024);
      dma16(p.v, vo, sm + L_R + (9 + w) * 1024);
      dma16(p.v, vo + 32768, sm + L_R + (13 + w) * 1024);
      if (w == 1) dma16(p.gate_w, (l * 16 + rot1 * 128) & 1023, sm + L_GW);
    }
    const int cp = l & 7, rg = l >> 3, c0 = ct * 64 + w * 16 + cp * 2;
    u32x4 sk;
    asm volatile("global_load_dwordx4 %0, %1, %2" : "=v"(sk) : "v"(c0 * 8), "s"(p.shift_k) : "memory");
    u32x2 bb[6];
#pragma unroll
    for (int fb = 0; fb < 6; ++fb) {
      int f = st * 32 + fb * 16 + 4 * g;
      asm volatile("global_load_dwordx2 %0, %1, %2" : "=v"(bb[fb]) : "v"((f < F_ ? f : 0) * 2), "s"(p.b1) : "memory");
    }
    if (wid == 0 && l < 2) *(LAS u32x4*)(cntC + l * 16) = u32x4{0, 0, 0, 0};
    wait_vm<0>();
    asm volatile("" : "+v"(sk), "+v"(bb[0]), "+v"(bb[1]), "+v"(bb[2]), "+v"(bb[3]), "+v"(bb[4]), "+v"(bb[5]));
    const u64 tag = bar_epoch(epoch) + 1;
    TS(1);
    {
      const u32 col = (w * 2 + (cp >> 2)) * 16, in = (cp & 3) * 4;
      const u32 a0 = sm + L_R + rg * 1024 + ((col ^ ((rg & 3) * 32)) + in);
      const u32 a1 = sm + L_R + (rg + 1) * 1024 + ((col ^ (((rg + 1) & 3) * 32)) + in);
      u32 kk[11], vv[8];
#pragma unroll
      for (int i = 0; i < 3; ++i) kk[i] = lds32(a0 + (5 + i) * 128);
#pragma unroll
      for (int i = 0; i < 8; ++i) kk[3 + i] = lds32(a1 + i * 128);
#pragma unroll
      for (int i = 0; i < 8; ++i) vv[i] = lds32(a0 + 9 * 1024 + i * 128);
      SB();
      const f32x2 s0 = {blo(sk.x), blo(sk.z)}, s1 = {bhi(sk.x), bhi(sk.z)}, s2 = {blo(sk.y), blo(sk.w)}, s3 = {bhi(sk.y), bhi(sk.w)};
      f32x2 kf[11], x[8];
#pragma unroll
      for (int i = 0; i < 11; ++i) kf[i] = unpk((i < 3 && r2 == 0 && rg == 0) ? 0u : kk[i]);
#pragma unroll
      for (int i = 0; i < 8; ++i)
        x[i] = __builtin_elementwise_fma(kf[i + 3], s0, __builtin_elementwise_fma(kf[i + 2], s1, __builtin_elementwise_fma(kf[i + 1], s2, kf[i] * s3))) * unpk(vv[i]);
      u32x4 o0 = {pk(x[0].x, x[1].x), pk(x[2].x, x[3].x), pk(x[4].x, x[5].x), pk(x[6].x, x[7].x)};
      u32x4 o1 = {pk(x[0].y, x[1].y), pk(x[2].y, x[3].y), pk(x[4].y, x[5].y), pk(x[6].y, x[7].y)};
      const u32 off = (u32)WS_KVT + c0 * 4096 + (r2 + rg * 8) * 2;
      __builtin_amdgcn_raw_buffer_store_b128(o0, rws, off, 0, 16);
      __builtin_amdgcn_raw_buffer_store_b128(o1, rws, off + 4096, 0, 16);
    }
    SB();
    u32x4 o1[2]; u32x2 o2[2];
#pragma unroll
    for (int i = 0; i < 2; ++i) {
      const int a = hf * 64 + l + 128 * i;
      asm volatile("global_load_dwordx4 %0, %1, %2" : "=v"(o1[i]) : "v"(chan * 4096 + (2040 - 8 * a) * 2), "s"(p.ssm_k) : "memory");
      asm volatile("global_load_dwordx2 %0, %1, %2" : "=v"(o2[i]) : "v"(chan * 4096 + (a == 0 ? 0 : (2048 - 8 * a) * 2)), "s"(p.ssm_k) : "memory");
    }
    auto rbuild = [&](int i) {
      u32 d[6];
      if (i < 2) { d[0] = o1[i].x; d[1] = o1[i].y; d[2] = o1[i].z; d[3] = o1[i].w; d[4] = o2[i].x; d[5] = o2[i].y; }
      else { d[0] = d[1] = d[2] = d[3] = 0; d[4] = l == 0 ? o2[0].x : 0; d[5] = l == 0 ? o2[0].y : 0; }
      u32 ro[6], mx[5];
#pragma unroll
      for (int t = 1; t < 6; ++t) { ro[t] = __builtin_amdgcn_alignbit(d[t], d[t], 16); mx[t - 1] = (d[t] & 0xffffu) | (d[t - 1] & 0xffff0000u); }
      const u32 a = sm + L_R + ch * RCH + (i < 2 ? hf * 64 + l + 128 * i : 256 + l) * 16;
      if (i < 2 || (hf == 0 && l < 2)) {
        *(LAS u32x4*)(a) = u32x4{mx[3], mx[2], mx[1], mx[0]};
        *(LAS u32x4*)(a + RP) = u32x4{ro[4], ro[3], ro[2], ro[1]};
        *(LAS u32x4*)(a + 2 * RP) = u32x4{mx[4], mx[3], mx[2], mx[1]};
        *(LAS u32x4*)(a + 3 * RP) = u32x4{ro[5], ro[4], ro[3], ro[2]};
      }
    };
    { const int ln = b * 43 + w * 11 + (l < 11 - (w == 3) ? l : 0);
      asm volatile("s_mov_b32 m0, %2\n\ts_nop 0\n\tglobal_load_lds_dword %0, %1" :: "v"(ln * 128), "s"(p.w2), "s"(sm + L_GP + 512) : "memory"); }
    TS(2);
    f32x4 acc[6][2];
#pragma unroll
    for (int fb = 0; fb < 6; ++fb) { f32x4 t = {blo(bb[fb].x), bhi(bb[fb].x), blo(bb[fb].y), bhi(bb[fb].y)}; acc[fb][0] = t; acc[fb][1] = t; }
    const int xr = w * 32 + i16;
    const u32 xa0 = sm + xr * 128 + ((g ^ ((xr >> 1) & 7)) * 16);
    const u32 wa0 = sm + L_W1 + (8 * g + (i16 >> 2)) * 192 + (l & 3) * 8 + (g & 1) * 32;
    const u32 wa1 = wa0 + 32 - (g & 1) * 64;
    u32x4 wf[17][7], xf[17][2];
    f32x4 ga[2] = {f32x4{0, 0, 0, 0}, f32x4{0, 0, 0, 0}};
    u32 xb[2][2] = {{xa0, xa0 ^ 64}, {xa0 + 2 * SLOT, (xa0 ^ 64) + 2 * SLOT}}, wb[2][2] = {{wa0, wa1}, {wa0 + 2 * SLOT, wa1 + 2 * SLOT}};
    u32 gwa = sm + L_GW + g * 16;
    asm volatile("" : "+v"(xb[0][0]), "+v"(xb[0][1]), "+v"(xb[1][0]), "+v"(xb[1][1]), "+v"(gwa));
    asm volatile("" : "+v"(wb[0][0]), "+v"(wb[0][1]), "+v"(wb[1][0]), "+v"(wb[1][1]));
    auto rd = [&](int k, int i) {
      const int hi = (k >> 2) & 1, so = ((k >> 1) & 1) * SLOT;
      if (i == 0) { xf[k][0] = lds128(xb[hi][k & 1] + so); xf[k][1] = lds128(xb[hi][k & 1] + so + 2048); }
      else if (i < 7) { const u32 a = wb[hi][(i - 1) & 1] + so + (k & 1) * 6144 + ((i - 1) >> 1) * 64; wf[k][i - 1] = cat(ldstr(a), ldstr(a + 768)); }
      else wf[k][6] = lds128(gwa + k * 64);
    };
    bar();
#pragma unroll
    for (int i = 0; i < 8; ++i) rd(0, i);
#pragma unroll
    for (int k = 0; k < 16; ++k) {
#pragma unroll
      for (int i = 0; i < 14; ++i) {
        if (k < 15 && i < 8) rd(k + 1, i);
        SB();
        if (i < 12) acc[i >> 1][i & 1] = mfma(wf[k][i >> 1], xf[k][i & 1], acc[i >> 1][i & 1]);
        else ga[i & 1] = mfma(wf[k][6], xf[k][i & 1], ga[i & 1]);
        SB();
      }
      if (k == 2 * PUBA) {
        wait_vm<0>();
        if (l == 0) publish(p.ws + WS_FA + ((ct * 4 + w) * 32 + (r2 >> 6)) * 8, tag);
      }
      if (k == 2 * PUBA + 2) { asm volatile("" : "+v"(o1[0]), "+v"(o2[0])); rbuild(0); rbuild(2); }
      if (k == 2 * PUBA + 4) { asm volatile("" : "+v"(o1[1]), "+v"(o2[1])); rbuild(1); }
      if (!(k & 1)) bar();
      if (k == 0) TS(16);
    }
    if (ch == rh && l < 16) { *(LAS float*)(sm + L_GP + (hf * 32 + l) * 4) = ga[0].x; *(LAS float*)(sm + L_GP + (hf * 32 + 16 + l) * 4) = ga[1].x; }
    TS(3);
    if (GARR < 0) lds_arrive(cntG);
    __builtin_amdgcn_s_setprio(PRF);
#pragma unroll
    for (int u = 0; u < 3; ++u) {
      if (u < nst) {
#pragma unroll
        for (int sb = 0; sb < 2; ++sb) {
          f32x4 a = acc[2 * u][sb], c = acc[2 * u + 1][sb];
          u32x4 o = {gelu2(a.x, a.y), gelu2(a.z, a.w), gelu2(c.x, c.y), gelu2(c.z, c.w)};
          __builtin_amdgcn_raw_buffer_store_b128(o, rws, (u32)WS_H + ((cl * 8 + w * 2 + sb) * NSTEP + st + u) * 1024 + l * 16, 0, 16);
          SB();
        }
      }
      if (u == GARR) lds_arrive(cntG);
    }
    TS(4);
    const u32 row2 = 4 * g + (i16 >> 2);
    u32 va[4], pa[4], hv[11];
#pragma unroll
    for (int nb = 0; nb < 4; ++nb) { va[nb] = sm + w * SLOT + row2 * 128 + (l & 3) * 8 + ((nb ^ ((row2 >> 1) & 3)) * 32); pa[nb] = sm + w * SLOT + i16 * 256 + (((4 * nb + g) ^ (l & 7)) * 16); asm volatile("" : "+v"(va[nb]), "+v"(pa[nb])); }
#pragma unroll
    for (int i = 0; i < 11; ++i) {
      const int t = step2(i);
      hv[i] = (u32)WS_H + ((cl * 8 + rh * 4) * NSTEP + w * 11) * 1024 + l * 16 + ((t == 10 && w == 3) ? 0x80000000u : t * 1024u);
      asm volatile("" : "+v"(hv[i]));
    }
    f32x4 ac2[4][4];
#pragma unroll
    for (int i = 0; i < 16; ++i) { ac2[i >> 2][i & 3] = f32x4{0, 0, 0, 0}; asm volatile("" : "+v"(ac2[i >> 2][i & 3])); }
    if (b == 0 && wid == 0) {
#pragma unroll 1
      for (int it = 0; it < SPIN_MAX; ++it) {
        bool ok = true;
#pragma unroll
        for (int i = 0; i < 4; ++i)
          ok &= __hip_atomic_load((GAS u64*)(p.ws + WS_FA + ((i * 2 + (l >> 5)) * 128 + (l & 31)) * 8), __ATOMIC_RELAXED, __HIP_MEMORY_SCOPE_AGENT) == tag;
        if (__all(ok)) break;
      }
    }
    if (NPRE < 0) { wait_vm<0>(); if (l == 0) publish(p.ws + WS_FH + (((cl * 2 + (w >> 1)) * 16 + mem) * 2 + (w & 1)) * 8, tag); TS(11); }
    if (b == 0 && threadIdx.x == 0) publish(p.ws + WS_EPOCH, tag);
    lds_wait(cntW, 4);
    TS(5);
    u32x4 wr[11][4];
#pragma unroll
    for (int i = 0; i < 4; ++i)
#pragma unroll
      for (int nb = 0; nb < 4; ++nb) wr[i][nb] = cat(ldstr(va[nb] + i * 4096), ldstr(va[nb] + i * 4096 + 2048));
#pragma unroll
    for (int i = 0; i < 4; ++i)
#pragma unroll
      for (int nb = 0; nb < 4; ++nb) asm volatile("" : "+v"(wr[i][nb]));
    TS(13);
    const u32 vo = (l >> 3) * 1024 + ct * 128 + (((l & 7) ^ (2 * ((l >> 4) & 3))) * 16);
    auto refill = [&](int i) {
      const int row = (w * 11 + step2(7 + (i >> 2))) * 32;
      dma16(p.w2 + ((row < F_ ? row : 0) + (i & 3) * 8) * 1024, vo, sm + w * SLOT + i * 1024);
    };
#pragma unroll
    for (int i = 0; i < 16; ++i) {
      if (i == NPRE) { TS(10); wait_vm<(NPRE < 0 ? 0 : NPRE)>(); if (l == 0) publish(p.ws + WS_FH + (((cl * 2 + (w >> 1)) * 16 + mem) * 2 + (w & 1)) * 8, tag); TS(11); }
      refill(i);
    }
    TS(6);
    poll<HSL>(p.ws + WS_FH + (((cl * 2 + rh) * 16 + w * 4) * 2 + (l & 7)) * 8, tag);
    TS(7);
#define GI(j) ((j) < 32 ? (j) >> 2 : 8 + ((j) - 32) % 3)
#define GS(j) ((j) < 32 ? (j) & 3 : ((j) - 32) / 3)
    u32x4 hf[44];
#pragma unroll
    for (int j = 0; j < WIN; ++j) hf[j] = __builtin_amdgcn_raw_buffer_load_b128(rws, hv[GI(j)], GS(j) * NSTEP * 1024, HAUX);
#pragma unroll
    for (int j = 0; j < 44; ++j) {
      const int i = GI(j), sb = GS(j), jn = j - 1 + WIN;
      if (j == ZTRIG) lds_arrive(cntG);
#pragma unroll
      for (int nb = 0; nb < 4; ++nb) {
        SB();
        asm volatile("v_mfma_f32_16x16x32_bf16 %0, %1, %2, %0" : "+v"(ac2[nb][sb]) : "v"(wr[i][nb]), "v"(hf[j]));
        SB();
        if (nb == 0 && j > 0 && jn < 44) hf[jn] = __builtin_amdgcn_raw_buffer_load_b128(rws, hv[GI(jn)], GS(jn) * NSTEP * 1024, HAUX);
        if ((nb == 1 || nb == 2) && i >= 1 && i < 8) {
          const u32x2 t = ldstr(va[sb] + (i + 3 < 7 ? i + 3 : i + 3 - 7) * 4096 + (nb - 1) * 2048);
          if (nb == 1) { wr[i + 3][sb].x = t.x; wr[i + 3][sb].y = t.y; } else { wr[i + 3][sb].z = t.x; wr[i + 3][sb].w = t.y; }
        }
        const int m = (i - 8) * 4 + nb;
        if (j >= 35 && m % 3 == 1) *(LAS f32x4*)(pa[m / 3] + (sb - 1) * 4096) = ac2[m / 3][sb - 1];
      }
    }
    asm volatile("s_nop 7\n\ts_nop 7" ::: "memory");
    TS(8);
#pragma unroll
    for (int nb = 0; nb < 4; ++nb) *(LAS f32x4*)(pa[nb] + 3 * 4096) = ac2[nb][3];
    bar();
    TS(9); TC(31); TSEND();
  } else {
    const int tc = w * 64 + l;
    u32 vx[4], vw[3];
#pragma unroll
    for (int i = 0; i < 4; ++i) { int r = (w * 4 + i) * 8 + (l >> 3); vx[i] = (r1 + r) * 1024 + (((l & 7) ^ ((r >> 1) & 7)) * 16); }
#pragma unroll
    for (int i = 0; i < 3; ++i) {
      int idx = (w * 3 + i) * 64 + l, r = idx / 12, c = (idx - r * 12) ^ (2 * ((r >> 3) & 1));
      if (c >= nst * 4) c -= 4;
      vw[i] = r * (F_ * 2) + st * 64 + c * 16;
    }
    const u32 vo2 = (l >> 3) * 1024 + ct * 128 + (((l & 7) ^ (2 * ((l >> 4) & 3))) * 16);
    int r7[7];
#pragma unroll
    for (int i = 0; i < 7; ++i) r7[i] = step2((w * 7 + i) >> 2) * 32 + ((w * 7 + i) & 3) * 8;
    auto issue = [&](int from, int to) {
#pragma unroll
      for (int n = from; n < to; ++n) {
        const int s = n / 7, i = n % 7;
        const u32 so = sm + (s & 3) * SLOT;
        const int sr = (s + rot1) & 7, row = (s & 3) * 11 * 32 + r7[i];
        if (s >= 8) dma16(p.w2 + (s == 11 && row >= F_ ? 0 : row) * 1024, vo2, so + (w * 7 + i) * 1024);
        else if (i < 4) dma16(p.q + sr * 128, vx[i], so + (w * 4 + i) * 1024);
        else dma16(p.w1 + sr * (64 * F_ * 2), vw[i - 4], so + L_W1 + (w * 3 + i - 4) * 1024);
      }
    };
    issue(0, 7);
    u32 dsk; u32x4 b2v;
    asm volatile("global_load_ushort %0, %1, %2" : "=v"(dsk) : "v"(chan * 2), "s"(p.ssm_D) : "memory");
    asm volatile("global_load_dwordx4 %0, %1, %2" : "=v"(b2v) : "v"(ct * 128 + (l & 7) * 16), "s"(p.b2) : "memory");
    issue(7, PREA);
    if (l < 32 && hf == 0) *(LAS u32x4*)(sm + L_U + ch * UCH + l * 16) = u32x4{0, 0, 0, 0};
    const u64 tag = bar_epoch(epoch) + 1;
    issue(PREA, 21);
    const int er = tc >> 3, ec = tc & 7;
    const int sq = (ct - rot1) & 7;
    const u32 ga = sm + (sq & 3) * SLOT + (rh * 64 + er) * 128 + ((ec ^ ((er >> 1) & 7)) * 16);
    u32x4 q0 = {0, 0, 0, 0}, q1 = {0, 0, 0, 0};
    const u32 fao = (u32)WS_FA + ((chan >> 4) * 32 + hf * 16 + i16) * 8, kvo = (u32)WS_KVT + chan * 4096 + hf * 2048 + l * 16;
    const u32 ub = sm + L_U + ch * UCH + 512 + hf * 2048 + l * 16;
    u64 fa = 0; u32x4 u0 = {0, 0, 0, 0}, u1 = {0, 0, 0, 0};
    bool early = false;
#define POS(s) ((s) == 0 ? 21 : (s) == 1 ? 14 + (14 + LAG) / 2 : 7 * (s) + 7 + LAG)
#define EXTRA(s, at, n) (((s) > (at) && POS(at) >= 7 * (s) + 7) ? (n) : 0)
    auto step = [&](auto sc) {
      constexpr int s = sc.value;
      if constexpr (s == SMP + 2) wait_vm<(POS(s) - POS(SMP) < POS(s) - 7 * s - 7 ? POS(s) - POS(SMP) : POS(s) - 7 * s - 7)>();
      else if constexpr (s < 8) wait_vm<POS(s) - 7 * s - 7 + EXTRA(s, SMP, 1) + EXTRA(s, SMP + 2, 2)>();
      bar();
      if (s == sq) { q0 = lds128(ga); q1 = lds128(ga + 4096); }
      if constexpr (s == SMP) asm volatile("global_load_dwordx2 %0, %1, %2 sc1" : "=v"(fa) : "v"(fao), "s"(p.ws) : "memory");
      if constexpr (s == SMP + 2) {
        asm volatile("" : "+v"(fa));
        early = __all(fa == tag);
        asm volatile("global_load_dwordx4 %0, %1, %2 sc1" : "=v"(u0) : "v"(early ? kvo : l * 16), "s"(p.ws) : "memory");
        asm volatile("global_load_dwordx4 %0, %1, %2 offset:1024 sc1" : "=v"(u1) : "v"(early ? kvo : l * 16), "s"(p.ws) : "memory");
      }
      issue(POS(s), s < 8 ? POS(s + 1) : 84);
    };
    step(IC<0>{}); step(IC<1>{}); step(IC<2>{}); step(IC<3>{}); step(IC<4>{}); step(IC<5>{}); step(IC<6>{}); step(IC<7>{}); step(IC<8>{});
    asm volatile("" : "+v"(dsk), "+v"(b2v), "+v"(q0), "+v"(q1));
    __builtin_amdgcn_s_setprio(PRC);
    TS(1);
    u32 ra = sm + L_R + ch * RCH + (i16 & 3) * RP + (2032 - 1792 - 32 * (4 * hf + 3) + 8 * g - (i16 & 12)) * 2;
    u32 rb2 = ra + 8, ua = sm + L_U + ch * UCH + (256 + 16 * i16 + 8 * g - 16 - 32 * (4 * hf + 3)) * 2;
    asm volatile("" : "+v"(ra), "+v"(rb2), "+v"(ua));
    u32x4 A[4][8], B[4][8];
#pragma unroll
    for (int n = 0; n < 8; ++n) { A[0][n] = cat(lds64(ra + (7 - n) * 512), lds64(rb2 + (7 - n) * 512)); SB(); }
    wait_vm<84 - POS(SMP + 2)>();
    asm volatile("" : "+v"(u0), "+v"(u1));
    if (!early) {
      poll(p.ws + fao, tag);
      u0 = __builtin_amdgcn_raw_buffer_load_b128(rws, kvo, 0, 16); u1 = __builtin_amdgcn_raw_buffer_load_b128(rws, kvo + 1024, 0, 16);
      wait_vm<0>();
      asm volatile("" : "+v"(u0), "+v"(u1));
      TS(10);
    }
    *(LAS u32x4*)ub = u0; *(LAS u32x4*)(ub + 1024) = u1;
    lds_arrive(cntP); lds_wait(cntP, 2);
    TS(2);
    f32x4 y[8];
#pragma unroll
    for (int n = 0; n < 8; ++n) { B[0][n] = lds128(ua + n * 512); y[n] = f32x4{0, 0, 0, 0}; }
#pragma unroll
    for (int cc = 0; cc < 4; ++cc) {
      if (cc == WARR) { wait_vm<0>(); lds_arrive(cntW); }
      if (cc == 0) { lds_wait(cntG, 4); TS(12); }
      if (cc == 3) lds_arrive(cntP);
#pragma unroll
      for (int pp = 0; pp < 8; ++pp)
#pragma unroll
        for (int n = pp; n < 8; ++n) {
          SB();
          y[n] = mfma(A[cc][pp], B[cc][n - pp], y[n]);
          SB();
          const int m = pp * 8 - pp * (pp - 1) / 2 + n - pp - 6;
          if (cc < 3 && m >= 0 && m < 24) {
            const int r = m / 3, o = (cc + 1) * 64;
            if (m % 3 == 0) { u32x2 t = lds64(ra + (7 - r) * 512 + o); A[cc + 1][r].x = t.x; A[cc + 1][r].y = t.y; }
            if (m % 3 == 1) { u32x2 t = lds64(rb2 + (7 - r) * 512 + o); A[cc + 1][r].z = t.x; A[cc + 1][r].w = t.y; }
            if (m % 3 == 2) B[cc + 1][r] = lds128(ua + r * 512 + o);
          }
        }
    }
    SB();
    TS(3);
    __builtin_amdgcn_s_setprio(PRZ);
    lds_wait(cntP, 4);
#pragma unroll
    for (int n = 0; n < 4; ++n) {
      const f32x4 lo = y[n], hi = y[n + 4];
      *(LAS f32x4*)(sm + L_R + ch * RCH + (hf * 4 + n) * 1024 + l * 16) = sel4(hf, lo, hi);
      y[n] = sel4(hf, hi, lo);
    }
    lds_arrive(cntP); lds_wait(cntP, 6);
    {
      u32x2 o[4];
      const float dk = blo(dsk);
#pragma unroll
      for (int n = 0; n < 4; ++n) {
        const f32x4 t = *(LAS f32x4*)(sm + L_R + ch * RCH + ((hf ^ 1) * 4 + n) * 1024 + l * 16);
        u32x2 uu = lds64(sm + L_U + ch * UCH + 512 + ((hf * 4 + n) * 256 + 16 * i16 + 4 * g) * 2);
        o[n] = u32x2{pk(add1(t.x, fma1(blo(uu.x), dk, y[n].x)), add1(t.y, fma1(bhi(uu.x), dk, y[n].y))),
                     pk(add1(t.z, fma1(blo(uu.y), dk, y[n].z)), add1(t.w, fma1(bhi(uu.y), dk, y[n].w)))};
      }
#pragma unroll
      for (int n = 0; n < 4; n += 2) {
        auto sx = __builtin_amdgcn_permlane16_swap(o[n].x, o[n + 1].x, false, false);
        auto sy = __builtin_amdgcn_permlane16_swap(o[n].y, o[n + 1].y, false, false);
        const int t0 = (hf * 4 + n + (g & 1)) * 256 + 16 * i16 + 8 * (g >> 1);
        __builtin_amdgcn_raw_buffer_store_b128(u32x4{sx[0], sy[0], sx[1], sy[1]}, rws, (u32)WS_ZT + chan * 4096 + t0 * 2, 0, 16);
      }
    }
    TS(11);
    wait_vm<0>();
    if (l == 0) publish(p.ws + WS_FZ + (hf * 512 + chan) * 8, tag);
    TS(4);
    lds_arrive(cntG); lds_wait(cntG, 12);
    poll<ZSL>(p.ws + WS_FZ + ((r2 >> 10) * 512 + ct * 64 + (l >> 5) * 32 + w * 8 + (l & 7)) * 8, tag);
    TS(5);
    const u32 zb = sm + L_R;
#pragma unroll
    for (int i = 0; i < 2; ++i) {
      u32x4 z = __builtin_amdgcn_raw_buffer_load_b128(rws, (u32)WS_ZT + (ct * 64 + er + 32 * i) * 4096 + r2 * 2 + ec * 16, 0, 16);
#pragma unroll
      for (int t = 0; t < 4; ++t) *(LAS u32*)(zb + (er + 32 * i) * 132 + ec * 16 + t * 4) = z[t];
    }
    const float gs0 = *(LAS float*)(sm + L_GP + er * 4), gs1 = *(LAS float*)(sm + L_GP + 128 + er * 4);
    const float g0 = __builtin_amdgcn_rcpf(1.0f + __builtin_amdgcn_exp2f(-1.442695041f * gs0));
    const float g1 = __builtin_amdgcn_rcpf(1.0f + __builtin_amdgcn_exp2f(-1.442695041f * gs1));
    lds_arrive(cntC); lds_wait(cntC, 4);
    float tt[2][8];
#pragma unroll
    for (int i = 0; i < 8; ++i) {
      const u32 a = zb + (ec * 8 + i) * 132 + er * 2;
      const float z0 = blo(*(LAS unsigned short*)a), z1 = blo(*(LAS unsigned short*)(a + 64));
      const float bb = (i & 1) ? bhi(b2v[i >> 1]) : blo(b2v[i >> 1]);
      tt[0][i] = fma1(mul1(g0, (i & 1) ? bhi(q0[i >> 1]) : blo(q0[i >> 1])), z0, mul1(1.0f - g0, bb));
      tt[1][i] = fma1(mul1(g1, (i & 1) ? bhi(q1[i >> 1]) : blo(q1[i >> 1])), z1, mul1(1.0f - g1, bb));
    }
#pragma unroll
    for (int i = 0; i < 8; ++i) asm volatile("" : "+v"(tt[0][i]), "+v"(tt[1][i]));
    TS(6);
    bar();
#pragma unroll
    for (int i = 0; i < 2; ++i) {
      const u32 a = sm + (er + 32 * i) * 256;
      f32x4 s0 = {0, 0, 0, 0}, s1 = {0, 0, 0, 0};
#pragma unroll
      for (int k = 0; k < 4; ++k) {
        s0 += *(LAS f32x4*)(a + k * SLOT + (((2 * ec) ^ (er & 7)) * 16));
        s1 += *(LAS f32x4*)(a + k * SLOT + (((2 * ec + 1) ^ (er & 7)) * 16));
      }
      const float m = 1.0f - (i ? g1 : g0);
      u32x4 o = {pk(tt[i][0] + m * s0.x, tt[i][1] + m * s0.y), pk(tt[i][2] + m * s0.z, tt[i][3] + m * s0.w),
                 pk(tt[i][4] + m * s1.x, tt[i][5] + m * s1.y), pk(tt[i][6] + m * s1.z, tt[i][7] + m * s1.w)};
      __builtin_amdgcn_raw_buffer_store_b128(o, __builtin_amdgcn_make_buffer_rsrc(p.out, 0, S_ * D_ * 2, 0x00020000), (r2 + er + 32 * i) * 1024 + ct * 128 + ec * 16, 0, OAUX);
    }
    TS(7); TSEND();
  }
}

extern "C" void kernel_launch(void* const* d_in, const int* in_sizes, int n_in, void* d_out, int out_size,
                              void* d_ws, size_t ws_size, hipStream_t stream) {
  Params p;
  p.q = (const char*)d_in[0]; p.k = (const char*)d_in[1]; p.v = (const char*)d_in[2];
  p.shift_k = (const char*)d_in[3]; p.ssm_k = (const char*)d_in[4]; p.ssm_D = (const char*)d_in[5];
  p.w1 = (const char*)d_in[7]; p.b1 = (const char*)d_in[8]; p.w2 = (const char*)d_in[9];
  p.b2 = (const char*)d_in[10]; p.gate_w = (const char*)d_in[11];
  p.out = (char*)d_out; p.ws = (char*)d_ws;
  (void)hipFuncSetAttribute((const void*)fused_kernel, hipFuncAttributeMaxDynamicSharedMemorySize, L_TOTAL);
  fused_kernel<<<256, 512, L_TOTAL, stream>>>(p);
}
```

```cpp
#include <hip/hip_runtime.h>
#include <hip/hip_bf16.h>

typedef unsigned u32;
typedef unsigned long long u64;
typedef __attribute__((ext_vector_type(2))) u32 u32x2;
typedef __attribute__((ext_vector_type(4))) u32 u32x4;
typedef __attribute__((ext_vector_type(4))) float f32x4;
typedef __attribute__((ext_vector_type(2))) float f32x2;
typedef __attribute__((ext_vector_type(8))) __bf16 bf16x8;
typedef __attribute__((ext_vector_type(2))) __bf16 bf16x2;
typedef __attribute__((ext_vector_type(4))) short s16x4;
#define LAS __attribute__((address_space(3)))
#define GAS __attribute__((address_space(1)))
#define DEV __device__ __forceinline__

constexpr int S_ = 2048, D_ = 512, F_ = 1376, NSTEP = 43;
constexpr size_t WS_EPOCH = 0, WS_FA = 8192, WS_FH = 16384, WS_FZ = 24576;
constexpr size_t WS_KVT = 65536, WS_ZT = WS_KVT + 2097152, WS_H = WS_ZT + 2097152;
constexpr u32 H_BYTES = 128u * NSTEP * 1024u;
constexpr size_t WS_END = WS_H + H_BYTES;
constexpr int SLOT = 28672, L_W1 = 16384;
constexpr int L_R = 4 * SLOT, RP = 4160, RCH = 4 * RP;
constexpr int L_U = L_R + 2 * RCH, UCH = 4608;
constexpr int L_GW = L_U + 2 * UCH;
constexpr int L_GP = L_GW + 1024;
constexpr int L_MISC = L_GP + 1024;
constexpr int L_TOTAL = L_MISC + 64;
constexpr int SPIN_MAX = 1 << 18;
template <int N> struct IC { static constexpr int value = N; };
constexpr int LAG = 11;
constexpr int KVK = 3;
constexpr int PUBA = 4;
constexpr int SMP = 6;
constexpr int SD = 1;
constexpr int DEF = 74;
constexpr int DS = 2;
constexpr int WQ = 56;
constexpr int WIN = 12;
constexpr int ZTRIG = 22;

struct Params {
  const char *q, *k, *v, *shift_k, *ssm_k, *ssm_D, *w1, *b1, *w2, *b2, *gate_w;
  char *out, *ws;
};

DEV void dma16(const char* sbase, u32 voff, u32 lds) {
  asm volatile("s_mov_b32 m0, %2\n\ts_nop 0\n\tglobal_load_lds_dwordx4 %0, %1" :: "v"(voff), "s"(sbase), "s"(lds) : "memory");
}
template <int N> DEV void wait_vm() { asm volatile("s_waitcnt vmcnt(%0)" :: "n"(N) : "memory"); }
DEV void bar() { asm volatile("s_waitcnt lgkmcnt(0)\n\ts_barrier" ::: "memory"); }
DEV u64 bar_epoch(u64 e) {
  u64 r;
  asm volatile("s_waitcnt lgkmcnt(0)\n\ts_barrier\n\ts_mov_b64 %0, %1" : "=s"(r) : "s"(e) : "memory");
  return r;
}
DEV u32x4 lds128(u32 a) { return *(LAS u32x4*)a; }
DEV u32x2 lds64(u32 a) { return *(LAS u32x2*)a; }
DEV u32 lds32(u32 a) { return *(LAS u32*)a; }
DEV u32x2 ldstr(u32 a) { return __builtin_bit_cast(u32x2, __builtin_amdgcn_ds_read_tr16_b64_v4i16((LAS s16x4*)a)); }
DEV u32x4 cat(u32x2 a, u32x2 b) { u32x4 r = {a.x, a.y, b.x, b.y}; return r; }
DEV f32x4 mfma(u32x4 a, u32x4 b, f32x4 c) {
  return __builtin_amdgcn_mfma_f32_16x16x32_bf16(__builtin_bit_cast(bf16x8, a), __builtin_bit_cast(bf16x8, b), c, 0, 0, 0);
}
DEV f32x4 sel4(int c, f32x4 a, f32x4 b) { f32x4 r = {c ? a.x : b.x, c ? a.y : b.y, c ? a.z : b.z, c ? a.w : b.w}; return r; }
DEV float blo(u32 x) { return __builtin_bit_cast(float, x << 16); }
DEV float bhi(u32 x) { return __builtin_bit_cast(float, x & 0xffff0000u); }
DEV u32 pk(float a, float b) { bf16x2 r = {(__bf16)a, (__bf16)b}; return __builtin_bit_cast(u32, r); }
DEV float add1(float a, float b) { float r = a + b; asm("" : "+v"(r)); return r; }
DEV float mul1(float a, float b) { float r = a * b; asm("" : "+v"(r)); return r; }
DEV float fma1(float a, float b, float c) { float r = __builtin_fmaf(a, b, c); asm("" : "+v"(r)); return r; }
DEV f32x2 unpk(u32 x) { f32x2 r = {blo(x), bhi(x)}; return r; }
DEV u32 gelu2(float a, float b) {
  const float c1 = -2.302208198f, c2 = -0.1029432397f;
  float t0 = a * a, t1 = b * b;
  asm("" : "+v"(t0)); asm("" : "+v"(t1));
  t0 = __builtin_fmaf(t0, c2, c1); t1 = __builtin_fmaf(t1, c2, c1);
  asm("" : "+v"(t0)); asm("" : "+v"(t1));
  t0 = __builtin_amdgcn_exp2f(a * t0); t1 = __builtin_amdgcn_exp2f(b * t1);
  t0 = __builtin_amdgcn_rcpf(1.0f + t0); t1 = __builtin_amdgcn_rcpf(1.0f + t1);
  t0 *= a; asm("" : "+v"(t0)); t1 *= b; asm("" : "+v"(t1));
  return pk(t0, t1);
}
#define SB() __builtin_amdgcn_sched_barrier(0)
DEV void publish(char* flag, u64 tag) { __hip_atomic_store((GAS u64*)flag, tag, __ATOMIC_RELAXED, __HIP_MEMORY_SCOPE_AGENT); }
template <int SL = 2> DEV void poll(const char* flag, u64 tag) {
#pragma unroll 1
  for (int i = 0; i < SPIN_MAX; ++i) {
    u64 x = __hip_atomic_load((GAS u64*)flag, __ATOMIC_RELAXED, __HIP_MEMORY_SCOPE_AGENT);
    if (__all(x == tag)) break;
    __builtin_amdgcn_s_sleep(SL);
  }
  asm volatile("" ::: "memory");
}
DEV void lds_arrive(u32 a) {
  if ((threadIdx.x & 63) == 0) asm volatile("ds_add_u32 %0, %1" :: "v"(a), "v"(1u) : "memory");
}
DEV void lds_wait(u32 a, u32 target) {
#pragma unroll 1
  for (int i = 0; i < SPIN_MAX; ++i) {
    u32 x = __hip_atomic_load((LAS u32*)a, __ATOMIC_RELAXED, __HIP_MEMORY_SCOPE_WORKGROUP);
    if (__builtin_amdgcn_readfirstlane(x) >= target) break;
    __builtin_amdgcn_s_sleep(1);
  }
  asm volatile("" ::: "memory");
}

__global__ void __launch_bounds__(512, 2) fused_kernel(Params p) {
  extern __shared__ __attribute__((aligned(16))) char smem[];
  const u32 sm = (u32)(size_t)(LAS char*)smem;
  const int b = blockIdx.x, l = threadIdx.x & 63;
  const char* pc; u32 clen;
  asm volatile("s_getpc_b64 %0\n.Lcode_a:\n\ts_mov_b32 %1, .Lcode_b-.Lcode_a" : "=s"(pc), "=s"(clen));
  {
    const u32 lines = ((b >> 3) & 3) * 8 + (threadIdx.x >> 6) + 32 * l;
    asm volatile("s_mov_b32 m0, %2\n\ts_nop 0\n\tglobal_load_lds_dword %0, %1" :: "v"(min(lines * 128u, clen)), "s"(pc), "s"(sm + L_GP + 512) : "memory");
    SB();
  }
  const int wid = __builtin_amdgcn_readfirstlane(threadIdx.x >> 6), w = wid & 3;
  const int xcd = b & 7, jj = b >> 3, cl = (xcd >> 1) * 4 + (jj >> 3);
  const int rh = xcd & 1, ct = jj & 7, mem = rh * 8 + ct;
  const int ch = w >> 1, hf = w & 1, chan = (xcd * 32 + jj) * 2 + ch;
  const int r1 = cl * 128, r2 = r1 + rh * 64;
  const int st = (mem >> 2) * 11 + (mem & 3) * 3 - ((mem + 1) >> 4);
  const int nst = ((mem & 3) == 3 || mem == 14) ? 2 : 3;
  const int rot = (ct + 2 * (jj >> 3)) & 7, rot1 = rot, rot2 = (rot * 11) >> 3;
  auto step2 = [&](int i) { const int t = i + rot2; return t >= 11 ? t - 11 : t; };
  const int i16 = l & 15, g = l >> 4;
  const u32 cntC = sm + L_MISC, cntW = cntC + 4, cntG = cntC + 8, cntP = cntC + 16 + ch * 4;
  u64 epoch;

  if (wid < 4) {
    const int cp = l & 7, rg = l >> 3, c0 = ct * 64 + w * 16 + cp * 2;
    const u32 vo1 = (r2 + w * 8 + (l >> 3)) * 1024 + ct * 128 + (((l & 7) ^ (2 * w)) * 16);
    u32x4 sk;
    auto kvload = [&]() {
      if (w == 0) dma16(p.k, vo1 + 57344, sm + L_R + 8 * 1024);
      dma16(p.k, (r2 == 0 && w == 0) ? vo1 : vo1 - 8192, sm + L_R + w * 1024);
      dma16(p.k, vo1 + 24576, sm + L_R + (w + 4) * 1024);
      dma16(p.v, vo1, sm + L_R + (9 + w) * 1024);
      dma16(p.v, vo1 + 32768, sm + L_R + (13 + w) * 1024);
      asm volatile("global_load_dwordx4 %0, %1, %2" : "=v"(sk) : "v"(c0 * 8), "s"(p.shift_k) : "memory");
    };
    if (w == 1) dma16(p.gate_w, (l * 16 + rot1 * 128) & 1023, sm + L_GW);
    u32x2 bb[6];
#pragma unroll
    for (int fb = 0; fb < 6; ++fb) {
      int f = st * 32 + fb * 16 + 4 * g;
      asm volatile("global_load_dwordx2 %0, %1, %2" : "=v"(bb[fb]) : "v"((f < F_ ? f : 0) * 2), "s"(p.b1) : "memory");
    }
    asm volatile("s_load_dwordx2 %0, %1, 0x0" : "=&s"(epoch), "+s"(p.ws) :: "memory");
    const __amdgpu_buffer_rsrc_t rws = __builtin_amdgcn_make_buffer_rsrc(p.ws, 0, (u32)WS_END, 0x00020000);
    auto touch = [&](const char* m) {
      const int ln = b * 43 + w * 11 + (l < 11 - (w == 3) ? l : 0);
      asm volatile("s_mov_b32 m0, %2\n\ts_nop 0\n\tglobal_load_lds_dword %0, %1" :: "v"(ln * 128), "s"(m), "s"(sm + L_GP + 512) : "memory");
    };
    kvload();
    if (wid == 0 && l < 2) *(LAS u32x4*)(cntC + l * 16) = u32x4{0, 0, 0, 0};
    wait_vm<5>();
    asm volatile("" : "+v"(bb[0]), "+v"(bb[1]), "+v"(bb[2]), "+v"(bb[3]), "+v"(bb[4]), "+v"(bb[5]));
    const u64 tag = bar_epoch(epoch) + 1;
    u32x4 o1[2]; u32x2 o2[2];
    auto ssmload = [&]() {
#pragma unroll
    for (int i = 0; i < 2; ++i) {
      const int a = hf * 64 + l + 128 * i;
      asm volatile("global_load_dwordx4 %0, %1, %2" : "=v"(o1[i]) : "v"(chan * 4096 + (2040 - 8 * a) * 2), "s"(p.ssm_k) : "memory");
      asm volatile("global_load_dwordx2 %0, %1, %2" : "=v"(o2[i]) : "v"(chan * 4096 + (a == 0 ? 0 : (2048 - 8 * a) * 2)), "s"(p.ssm_k) : "memory");
    }
    };
    auto kvblock = [&]() {
    asm volatile("" : "+v"(sk));
    {
      const u32 col = (w * 2 + (cp >> 2)) * 16, in = (cp & 3) * 4;
      const u32 a0 = sm + L_R + rg * 1024 + ((col ^ ((rg & 3) * 32)) + in);
      const u32 a1 = sm + L_R + (rg + 1) * 1024 + ((col ^ (((rg + 1) & 3) * 32)) + in);
      u32 kk[11], vv[8];
#pragma unroll
      for (int i = 0; i < 3; ++i) kk[i] = lds32(a0 + (5 + i) * 128);
#pragma unroll
      for (int i = 0; i < 8; ++i) kk[3 + i] = lds32(a1 + i * 128);
#pragma unroll
      for (int i = 0; i < 8; ++i) vv[i] = lds32(a0 + 9 * 1024 + i * 128);
      SB();
      const f32x2 s0 = {blo(sk.x), blo(sk.z)}, s1 = {bhi(sk.x), bhi(sk.z)}, s2 = {blo(sk.y), blo(sk.w)}, s3 = {bhi(sk.y), bhi(sk.w)};
      f32x2 kf[11], x[8];
#pragma unroll
      for (int i = 0; i < 11; ++i) kf[i] = unpk((i < 3 && r2 == 0 && rg == 0) ? 0u : kk[i]);
#pragma unroll
      for (int i = 0; i < 8; ++i)
        x[i] = __builtin_elementwise_fma(kf[i + 3], s0, __builtin_elementwise_fma(kf[i + 2], s1, __builtin_elementwise_fma(kf[i + 1], s2, kf[i] * s3))) * unpk(vv[i]);
      u32x4 o0 = {pk(x[0].x, x[1].x), pk(x[2].x, x[3].x), pk(x[4].x, x[5].x), pk(x[6].x, x[7].x)};
      u32x4 o1 = {pk(x[0].y, x[1].y), pk(x[2].y, x[3].y), pk(x[4].y, x[5].y), pk(x[6].y, x[7].y)};
      const u32 off = (u32)WS_KVT + c0 * 4096 + (r2 + rg * 8) * 2;
      __builtin_amdgcn_raw_buffer_store_b128(o0, rws, off, 0, 16);
      __builtin_amdgcn_raw_buffer_store_b128(o1, rws, off + 4096, 0, 16);
    }
    SB();
    ssmload();
    };
    auto rbuild = [&](int i) {
      u32 d[6];
      if (i < 2) { d[0] = o1[i].x; d[1] = o1[i].y; d[2] = o1[i].z; d[3] = o1[i].w; d[4] = o2[i].x; d[5] = o2[i].y; }
      else { d[0] = d[1] = d[2] = d[3] = 0; d[4] = l == 0 ? o2[0].x : 0; d[5] = l == 0 ? o2[0].y : 0; }
      u32 ro[6], mx[5];
#pragma unroll
      for (int t = 1; t < 6; ++t) { ro[t] = __builtin_amdgcn_alignbit(d[t], d[t], 16); mx[t - 1] = (d[t] & 0xffffu) | (d[t - 1] & 0xffff0000u); }
      const u32 a = sm + L_R + ch * RCH + (i < 2 ? hf * 64 + l + 128 * i : 256 + l) * 16;
      if (i < 2 || (hf == 0 && l < 2)) {
        *(LAS u32x4*)(a) = u32x4{mx[3], mx[2], mx[1], mx[0]};
        *(LAS u32x4*)(a + RP) = u32x4{ro[4], ro[3], ro[2], ro[1]};
        *(LAS u32x4*)(a + 2 * RP) = u32x4{mx[4], mx[3], mx[2], mx[1]};
        *(LAS u32x4*)(a + 3 * RP) = u32x4{ro[5], ro[4], ro[3], ro[2]};
      }
    };
    touch(p.w2);
    f32x4 acc[6][2];
#pragma unroll
    for (int fb = 0; fb < 6; ++fb) { f32x4 t = {blo(bb[fb].x), bhi(bb[fb].x), blo(bb[fb].y), bhi(bb[fb].y)}; acc[fb][0] = t; acc[fb][1] = t; }
    const int xr = w * 32 + i16;
    const u32 xa0 = sm + xr * 128 + ((g ^ ((xr >> 1) & 7)) * 16);
    const u32 wa0 = sm + L_W1 + (8 * g + (i16 >> 2)) * 192 + (l & 3) * 8 + (g & 1) * 32;
    const u32 wa1 = wa0 + 32 - (g & 1) * 64;
    u32x4 wf[17][7], xf[17][2];
    f32x4 ga[2] = {f32x4{0, 0, 0, 0}, f32x4{0, 0, 0, 0}};
    u32 xb[2][2] = {{xa0, xa0 ^ 64}, {xa0 + 2 * SLOT, (xa0 ^ 64) + 2 * SLOT}}, wb[2][2] = {{wa0, wa1}, {wa0 + 2 * SLOT, wa1 + 2 * SLOT}};
    u32 gwa = sm + L_GW + g * 16;
    asm volatile("" : "+v"(xb[0][0]), "+v"(xb[0][1]), "+v"(xb[1][0]), "+v"(xb[1][1]), "+v"(gwa));
    asm volatile("" : "+v"(wb[0][0]), "+v"(wb[0][1]), "+v"(wb[1][0]), "+v"(wb[1][1]));
    auto rd = [&](int k, int i) {
      const int hi = (k >> 2) & 1, so = ((k >> 1) & 1) * SLOT;
      if (i == 0) { xf[k][0] = lds128(xb[hi][k & 1] + so); xf[k][1] = lds128(xb[hi][k & 1] + so + 2048); }
      else if (i < 7) { const u32 a = wb[hi][(i - 1) & 1] + so + (k & 1) * 6144 + ((i - 1) >> 1) * 64; wf[k][i - 1] = cat(ldstr(a), ldstr(a + 768)); }
      else wf[k][6] = lds128(gwa + k * 64);
    };
#pragma unroll
    for (int i = 0; i < 8; ++i) rd(0, i);
#pragma unroll
    for (int k = 0; k < 16; ++k) {
#pragma unroll
      for (int i = 0; i < 14; ++i) {
        if (k < 15 && i < 8) rd(k + 1, i);
        SB();
        if (i < 12) acc[i >> 1][i & 1] = mfma(wf[k][i >> 1], xf[k][i & 1], acc[i >> 1][i & 1]);
        else ga[i & 1] = mfma(wf[k][6], xf[k][i & 1], ga[i & 1]);
        SB();
      }
      if (k == KVK) { kvblock(); SB(); }
      if (k == 2 * PUBA) {
        wait_vm<0>();
        if (l == 0) publish(p.ws + WS_FA + ((ct * 4 + w) * 32 + (r2 >> 6)) * 8, tag);
      }
      if (k == 2 * PUBA + 2) { asm volatile("" : "+v"(o1[0]), "+v"(o2[0])); rbuild(0); rbuild(2); }
      if (k == 2 * PUBA + 4) { asm volatile("" : "+v"(o1[1]), "+v"(o2[1])); rbuild(1); }
      if (k == ((KVK - 1) & ~1)) wait_vm<1>();
      if (!(k & 1)) bar();
    }
    if (ch == rh && l < 16) { *(LAS float*)(sm + L_GP + (hf * 32 + l) * 4) = ga[0].x; *(LAS float*)(sm + L_GP + (hf * 32 + 16 + l) * 4) = ga[1].x; }
    lds_arrive(cntG);
    __builtin_amdgcn_s_setprio(1);
#pragma unroll
    for (int u = 0; u < 3; ++u) {
      if (u < nst) {
#pragma unroll
        for (int sb = 0; sb < 2; ++sb) {
          f32x4 a = acc[2 * u][sb], c = acc[2 * u + 1][sb];
          u32x4 o = {gelu2(a.x, a.y), gelu2(a.z, a.w), gelu2(c.x, c.y), gelu2(c.z, c.w)};
          __builtin_amdgcn_raw_buffer_store_b128(o, rws, (u32)WS_H + ((cl * 8 + w * 2 + sb) * NSTEP + st + u) * 1024 + l * 16, 0, 16);
          SB();
        }
      }
    }
    const u32 row2 = 4 * g + (i16 >> 2);
    u32 va[4], pa[4], hv[11];
#pragma unroll
    for (int nb = 0; nb < 4; ++nb) { va[nb] = sm + w * SLOT + row2 * 128 + (l & 3) * 8 + ((nb ^ ((row2 >> 1) & 3)) * 32); pa[nb] = sm + w * SLOT + i16 * 256 + (((4 * nb + g) ^ (l & 7)) * 16); asm volatile("" : "+v"(va[nb]), "+v"(pa[nb])); }
#pragma unroll
    for (int i = 0; i < 11; ++i) {
      const int t = step2(i);
      hv[i] = (u32)WS_H + ((cl * 8 + rh * 4) * NSTEP + w * 11) * 1024 + l * 16 + ((t == 10 && w == 3) ? 0x80000000u : t * 1024u);
      asm volatile("" : "+v"(hv[i]));
    }
    f32x4 ac2[4][4];
#pragma unroll
    for (int i = 0; i < 16; ++i) { ac2[i >> 2][i & 3] = f32x4{0, 0, 0, 0}; asm volatile("" : "+v"(ac2[i >> 2][i & 3])); }
    if (b == 0 && wid == 0) {
#pragma unroll 1
      for (int it = 0; it < SPIN_MAX; ++it) {
        bool ok = true;
#pragma unroll
        for (int i = 0; i < 4; ++i)
          ok &= __hip_atomic_load((GAS u64*)(p.ws + WS_FA + ((i * 2 + (l >> 5)) * 128 + (l & 31)) * 8), __ATOMIC_RELAXED, __HIP_MEMORY_SCOPE_AGENT) == tag;
        if (__all(ok)) break;
      }
    }
    { wait_vm<0>(); if (l == 0) publish(p.ws + WS_FH + (((cl * 2 + (w >> 1)) * 16 + mem) * 2 + (w & 1)) * 8, tag); }
    if (b == 0 && threadIdx.x == 0) publish(p.ws + WS_EPOCH, tag);
    lds_wait(cntW, 4);
    u32x4 wr[11][4];
#pragma unroll
    for (int i = 0; i < 4; ++i)
#pragma unroll
      for (int nb = 0; nb < 4; ++nb) wr[i][nb] = cat(ldstr(va[nb] + i * 4096), ldstr(va[nb] + i * 4096 + 2048));
#pragma unroll
    for (int i = 0; i < 4; ++i)
#pragma unroll
      for (int nb = 0; nb < 4; ++nb) asm volatile("" : "+v"(wr[i][nb]));
    const u32 vo = (l >> 3) * 1024 + ct * 128 + (((l & 7) ^ (2 * ((l >> 4) & 3))) * 16);
    auto refill = [&](int i) {
      const int row = (w * 11 + step2(7 + (i >> 2))) * 32;
      dma16(p.w2 + ((row < F_ ? row : 0) + (i & 3) * 8) * 1024, vo, sm + w * SLOT + i * 1024);
    };
#pragma unroll
    for (int i = 0; i < 16; ++i) {
      refill(i);
    }
    poll<2>(p.ws + WS_FH + (((cl * 2 + rh) * 16 + w * 4) * 2 + (l & 7)) * 8, tag);
#define GI(j) ((j) < 32 ? (j) >> 2 : 8 + ((j) - 32) % 3)
#define GS(j) ((j) < 32 ? (j) & 3 : ((j) - 32) / 3)
    u32x4 hf[44];
#pragma unroll
    for (int j = 0; j < WIN; ++j) hf[j] = __builtin_amdgcn_raw_buffer_load_b128(rws, hv[GI(j)], GS(j) * NSTEP * 1024, 16);
#pragma unroll
    for (int j = 0; j < 44; ++j) {
      const int i = GI(j), sb = GS(j), jn = j - 1 + WIN;
      if (j == ZTRIG) lds_arrive(cntG);
#pragma unroll
      for (int nb = 0; nb < 4; ++nb) {
        SB();
        asm volatile("v_mfma_f32_16x16x32_bf16 %0, %1, %2, %0" : "+v"(ac2[nb][sb]) : "v"(wr[i][nb]), "v"(hf[j]));
        SB();
        if (nb == 0 && j > 0 && jn < 44) hf[jn] = __builtin_amdgcn_raw_buffer_load_b128(rws, hv[GI(jn)], GS(jn) * NSTEP * 1024, 16);
        if ((nb == 1 || nb == 2) && i >= 1 && i < 8) {
          const u32x2 t = ldstr(va[sb] + (i + 3 < 7 ? i + 3 : i + 3 - 7) * 4096 + (nb - 1) * 2048);
          if (nb == 1) { wr[i + 3][sb].x = t.x; wr[i + 3][sb].y = t.y; } else { wr[i + 3][sb].z = t.x; wr[i + 3][sb].w = t.y; }
        }
        const int m = (i - 8) * 4 + nb;
        if (j >= 35 && m % 3 == 1) *(LAS f32x4*)(pa[m / 3] + (sb - 1) * 4096) = ac2[m / 3][sb - 1];
      }
    }
    asm volatile("s_nop 7\n\ts_nop 7" ::: "memory");
#pragma unroll
    for (int nb = 0; nb < 4; ++nb) *(LAS f32x4*)(pa[nb] + 3 * 4096) = ac2[nb][3];
    asm volatile(".Lcode_b:");
    bar();
  } else {
    const int tc = w * 64 + l;
    u32 vx[4], vw[3];
#pragma unroll
    for (int i = 0; i < 4; ++i) { int r = (w * 4 + i) * 8 + (l >> 3); vx[i] = (r1 + r) * 1024 + (((l & 7) ^ ((r >> 1) & 7)) * 16); }
#pragma unroll
    for (int i = 0; i < 3; ++i) {
      int idx = (w * 3 + i) * 64 + l, r = idx / 12, c = (idx - r * 12) ^ (2 * ((r >> 3) & 1));
      if (c >= nst * 4) c -= 4;
      vw[i] = r * (F_ * 2) + st * 64 + c * 16;
    }
    const u32 vo2 = (l >> 3) * 1024 + ct * 128 + (((l & 7) ^ (2 * ((l >> 4) & 3))) * 16);
    int r7[7];
#pragma unroll
    for (int i = 0; i < 7; ++i) r7[i] = step2((w * 7 + i) >> 2) * 32 + ((w * 7 + i) & 3) * 8;
    auto issue = [&](int from, int to) {
#pragma unroll
      for (int n = from; n < to; ++n) {
        const int s = n / 7, i = n % 7;
        const u32 so = sm + (s & 3) * SLOT;
        const int sr = (s + rot1) & 7, row = (s & 3) * 11 * 32 + r7[i];
        if (s >= 8) dma16(p.w2 + (s == 11 && row >= F_ ? 0 : row) * 1024, vo2, so + (w * 7 + i) * 1024);
        else if (i < 4) dma16(p.q + sr * 128, vx[i], so + (w * 4 + i) * 1024);
        else dma16(p.w1 + sr * (64 * F_ * 2), vw[i - 4], so + L_W1 + (w * 3 + i - 4) * 1024);
      }
    };
    issue(0, 7);
    asm volatile("s_load_dwordx2 %0, %1, 0x0" : "=&s"(epoch), "+s"(p.ws) :: "memory");
    const __amdgpu_buffer_rsrc_t rws = __builtin_amdgcn_make_buffer_rsrc(p.ws, 0, (u32)WS_END, 0x00020000);
    u32 dsk; u32x4 b2v;
    asm volatile("global_load_ushort %0, %1, %2" : "=v"(dsk) : "v"(chan * 2), "s"(p.ssm_D) : "memory");
    asm volatile("global_load_dwordx4 %0, %1, %2" : "=v"(b2v) : "v"(ct * 128 + (l & 7) * 16), "s"(p.b2) : "memory");
    issue(7, 11);
    if (l < 32 && hf == 0) *(LAS u32x4*)(sm + L_U + ch * UCH + l * 16) = u32x4{0, 0, 0, 0};
    issue(11, 21); wait_vm<14>();
    const u64 tag = bar_epoch(epoch) + 1;
    const int er = tc >> 3, ec = tc & 7;
    const int sq = (ct - rot1) & 7;
    const u32 ga = sm + (sq & 3) * SLOT + (rh * 64 + er) * 128 + ((ec ^ ((er >> 1) & 7)) * 16);
    u32x4 q0 = {0, 0, 0, 0}, q1 = {0, 0, 0, 0};
    const u32 fao = (u32)WS_FA + ((chan >> 4) * 32 + hf * 16 + i16) * 8, kvo = (u32)WS_KVT + chan * 4096 + hf * 2048 + l * 16;
    const u32 ub = sm + L_U + ch * UCH + 512 + hf * 2048 + l * 16;
    u64 fa = 0; u32x4 u0 = {0, 0, 0, 0}, u1 = {0, 0, 0, 0};
    bool early = false;
#define POS(s) ((s) == 0 ? 21 : (s) == 1 ? 14 + (14 + LAG) / 2 : 7 * (s) + 7 + LAG)
#define EXTRA(s, at, n) (((s) > (at) && POS(at) >= 7 * (s) + 7) ? (n) : 0)
    auto step = [&](auto sc) {
      constexpr int s = sc.value;
      if constexpr (s == SMP + SD) wait_vm<(POS(s) - POS(SMP) < POS(s) - 7 * s - 7 ? POS(s) - POS(SMP) : POS(s) - 7 * s - 7)>();
      else if constexpr (s < 8) wait_vm<POS(s) - 7 * s - 7 + EXTRA(s, SMP, 1) + EXTRA(s, SMP + SD, 2)>();
      if (s > 0) bar();
      if (s == sq) { q0 = lds128(ga); q1 = lds128(ga + 4096); }
      if constexpr (s == SMP) asm volatile("global_load_dwordx2 %0, %1, %2 sc1" : "=v"(fa) : "v"(fao), "s"(p.ws) : "memory");
      if constexpr (s == SMP + SD) {
        asm volatile("" : "+v"(fa));
        early = __all(fa == tag);
        asm volatile("global_load_dwordx4 %0, %1, %2 sc1" : "=v"(u0) : "v"(early ? kvo : l * 16), "s"(p.ws) : "memory");
        asm volatile("global_load_dwordx4 %0, %1, %2 offset:1024 sc1" : "=v"(u1) : "v"(early ? kvo : l * 16), "s"(p.ws) : "memory");
      }
      issue(POS(s), s < 8 ? POS(s + 1) : DEF);
    };
    step(IC<0>{}); step(IC<1>{}); step(IC<2>{}); step(IC<3>{}); step(IC<4>{}); step(IC<5>{}); step(IC<6>{}); step(IC<7>{}); step(IC<8>{});
    asm volatile("" : "+v"(dsk), "+v"(b2v), "+v"(q0), "+v"(q1));
    __builtin_amdgcn_s_setprio(0);
    u32 ra = sm + L_R + ch * RCH + (i16 & 3) * RP + (2032 - 1792 - 32 * (4 * hf + 3) + 8 * g - (i16 & 12)) * 2;
    u32 rb2 = ra + 8, ua = sm + L_U + ch * UCH + (256 + 16 * i16 + 8 * g - 16 - 32 * (4 * hf + 3)) * 2;
    asm volatile("" : "+v"(ra), "+v"(rb2), "+v"(ua));
    u32x4 A[4][8], B[4][8];
#pragma unroll
    for (int n = 0; n < 8; ++n) { A[0][n] = cat(lds64(ra + (7 - n) * 512), lds64(rb2 + (7 - n) * 512)); SB(); }
    wait_vm<DEF - POS(SMP + SD)>();
    asm volatile("" : "+v"(u0), "+v"(u1));
    if (!early) {
      poll(p.ws + fao, tag);
      u0 = __builtin_amdgcn_raw_buffer_load_b128(rws, kvo, 0, 16); u1 = __builtin_amdgcn_raw_buffer_load_b128(rws, kvo + 1024, 0, 16);
      wait_vm<0>();
      asm volatile("" : "+v"(u0), "+v"(u1));
    }
    *(LAS u32x4*)ub = u0; *(LAS u32x4*)(ub + 1024) = u1;
    lds_arrive(cntP); lds_wait(cntP, 2);
    f32x4 y[8];
#pragma unroll
    for (int n = 0; n < 8; ++n) { B[0][n] = lds128(ua + n * 512); y[n] = f32x4{0, 0, 0, 0}; }
#pragma unroll
    for (int cc = 0; cc < 4; ++cc) {
      if (cc == 0) { lds_wait(cntG, 4); }
      if (cc == 3) lds_arrive(cntP);
#pragma unroll
      for (int pp = 0; pp < 8; ++pp)
#pragma unroll
        for (int n = pp; n < 8; ++n) {
          SB();
          y[n] = mfma(A[cc][pp], B[cc][n - pp], y[n]);
          SB();
          const int m = pp * 8 - pp * (pp - 1) / 2 + n - pp - 6;
          if (cc == 0 && (m + 6) % DS == 0 && DEF + (m + 6) / DS < 84) issue(DEF + (m + 6) / DS, DEF + (m + 6) / DS + 1);
          if (cc * 36 + m + 6 == WQ) { wait_vm<0>(); lds_arrive(cntW); }
          if (cc < 3 && m >= 0 && m < 24) {
            const int r = m / 3, o = (cc + 1) * 64;
            if (m % 3 == 0) { u32x2 t = lds64(ra + (7 - r) * 512 + o); A[cc + 1][r].x = t.x; A[cc + 1][r].y = t.y; }
            if (m % 3 == 1) { u32x2 t = lds64(rb2 + (7 - r) * 512 + o); A[cc + 1][r].z = t.x; A[cc + 1][r].w = t.y; }
            if (m % 3 == 2) B[cc + 1][r] = lds128(ua + r * 512 + o);
          }
        }
    }
    SB();
    __builtin_amdgcn_s_setprio(2);
    lds_wait(cntP, 4);
#pragma unroll
    for (int n = 0; n < 4; ++n) {
      const f32x4 lo = y[n], hi = y[n + 4];
      *(LAS f32x4*)(sm + L_R + ch * RCH + (hf * 4 + n) * 1024 + l * 16) = sel4(hf, lo, hi);
      y[n] = sel4(hf, hi, lo);
    }
    lds_arrive(cntP); lds_wait(cntP, 6);
    {
      u32x2 o[4];
      const float dk = blo(dsk);
#pragma unroll
      for (int n = 0; n < 4; ++n) {
        const f32x4 t = *(LAS f32x4*)(sm + L_R + ch * RCH + ((hf ^ 1) * 4 + n) * 1024 + l * 16);
        u32x2 uu = lds64(sm + L_U + ch * UCH + 512 + ((hf * 4 + n) * 256 + 16 * i16 + 4 * g) * 2);
        o[n] = u32x2{pk(add1(t.x, fma1(blo(uu.x), dk, y[n].x)), add1(t.y, fma1(bhi(uu.x), dk, y[n].y))),
                     pk(add1(t.z, fma1(blo(uu.y), dk, y[n].z)), add1(t.w, fma1(bhi(uu.y), dk, y[n].w)))};
      }
#pragma unroll
      for (int n = 0; n < 4; n += 2) {
        auto sx = __builtin_amdgcn_permlane16_swap(o[n].x, o[n + 1].x, false, false);
        auto sy = __builtin_amdgcn_permlane16_swap(o[n].y, o[n + 1].y, false, false);
        const int t0 = (hf * 4 + n + (g & 1)) * 256 + 16 * i16 + 8 * (g >> 1);
        __builtin_amdgcn_raw_buffer_store_b128(u32x4{sx[0], sy[0], sx[1], sy[1]}, rws, (u32)WS_ZT + chan * 4096 + t0 * 2, 0, 16);
      }
    }
    wait_vm<0>();
    if (l == 0) publish(p.ws + WS_FZ + (hf * 512 + chan) * 8, tag);
    lds_arrive(cntG); lds_wait(cntG, 12);
    poll<8>(p.ws + WS_FZ + ((r2 >> 10) * 512 + ct * 64 + (l >> 5) * 32 + w * 8 + (l & 7)) * 8, tag);
    const u32 zb = sm + L_R;
#pragma unroll
    for (int i = 0; i < 2; ++i) {
      u32x4 z = __builtin_amdgcn_raw_buffer_load_b128(rws, (u32)WS_ZT + (ct * 64 + er + 32 * i) * 4096 + r2 * 2 + ec * 16, 0, 16);
#pragma unroll
      for (int t = 0; t < 4; ++t) *(LAS u32*)(zb + (er + 32 * i) * 132 + ec * 16 + t * 4) = z[t];
    }
    const float gs0 = *(LAS float*)(sm + L_GP + er * 4), gs1 = *(LAS float*)(sm + L_GP + 128 + er * 4);
    const float g0 = __builtin_amdgcn_rcpf(1.0f + __builtin_amdgcn_exp2f(-1.442695041f * gs0));
    const float g1 = __builtin_amdgcn_rcpf(1.0f + __builtin_amdgcn_exp2f(-1.442695041f * gs1));
    lds_arrive(cntC); lds_wait(cntC, 4);
    float tt[2][8];
#pragma unroll
    for (int i = 0; i < 8; ++i) {
      const u32 a = zb + (ec * 8 + i) * 132 + er * 2;
      const float z0 = blo(*(LAS unsigned short*)a), z1 = blo(*(LAS unsigned short*)(a + 64));
      const float bb = (i & 1) ? bhi(b2v[i >> 1]) : blo(b2v[i >> 1]);
      tt[0][i] = fma1(mul1(g0, (i & 1) ? bhi(q0[i >> 1]) : blo(q0[i >> 1])), z0, mul1(1.0f - g0, bb));
      tt[1][i] = fma1(mul1(g1, (i & 1) ? bhi(q1[i >> 1]) : blo(q1[i >> 1])), z1, mul1(1.0f - g1, bb));
    }
#pragma unroll
    for (int i = 0; i < 8; ++i) asm volatile("" : "+v"(tt[0][i]), "+v"(tt[1][i]));
    bar();
#pragma unroll
    for (int i = 0; i < 2; ++i) {
      const u32 a = sm + (er + 32 * i) * 256;
      f32x4 s0 = {0, 0, 0, 0}, s1 = {0, 0, 0, 0};
#pragma unroll
      for (int k = 0; k < 4; ++k) {
        s0 += *(LAS f32x4*)(a + k * SLOT + (((2 * ec) ^ (er & 7)) * 16));
        s1 += *(LAS f32x4*)(a + k * SLOT + (((2 * ec + 1) ^ (er & 7)) * 16));
      }
      const float m = 1.0f - (i ? g1 : g0);
      u32x4 o = {pk(tt[i][0] + m * s0.x, tt[i][1] + m * s0.y), pk(tt[i][2] + m * s0.z, tt[i][3] + m * s0.w),
                 pk(tt[i][4] + m * s1.x, tt[i][5] + m * s1.y), pk(tt[i][6] + m * s1.z, tt[i][7] + m * s1.w)};
      __builtin_amdgcn_raw_buffer_store_b128(o, __builtin_amdgcn_make_buffer_rsrc(p.out, 0, S_ * D_ * 2, 0x00020000), (r2 + er + 32 * i) * 1024 + ct * 128 + ec * 16, 0, 16);
    }
  }
}

extern "C" void kernel_launch(void* const* d_in, const int* in_sizes, int n_in, void* d_out, int out_size,
                              void* d_ws, size_t ws_size, hipStream_t stream) {
  Params p;
  p.q = (const char*)d_in[0]; p.k = (const char*)d_in[1]; p.v = (const char*)d_in[2];
  p.shift_k = (const char*)d_in[3]; p.ssm_k = (const char*)d_in[4]; p.ssm_D = (const char*)d_in[5];
  p.w1 = (const char*)d_in[7]; p.b1 = (const char*)d_in[8]; p.w2 = (const char*)d_in[9];
  p.b2 = (const char*)d_in[10]; p.gate_w = (const char*)d_in[11];
  p.out = (char*)d_out; p.ws = (char*)d_ws;
  (void)hipFuncSetAttribute((const void*)fused_kernel, hipFuncAttributeMaxDynamicSharedMemorySize, L_TOTAL);
  fused_kernel<<<256, 512, L_TOTAL, stream>>>(p);
}
```
